```python
import torch

def _softmax(x, axis=-1):
    return torch.softmax(x, dim=axis)
def _gelu(x):
    return torch.nn.functional.gelu(x, approximate="tanh")
def _silu(x):
    return torch.nn.functional.silu(x)
def _sigmoid(x):
    return torch.sigmoid(x)
def _layernorm(x, axis=-1, eps=1e-5):
    m = x.mean(dim=axis, keepdim=True)
    v = x.var(dim=axis, keepdim=True, unbiased=False)
    return (x - m) / torch.sqrt(v + eps)
def _scatter_add(tgt, idx, src, axis=0):
    return tgt.clone().index_add_(axis, idx, src)
def _segment_sum(src, seg, n):
    out = torch.zeros((n,) + src.shape[1:], dtype=src.dtype)
    return out.index_add_(0, seg, src)
def _pad(x, pw):
    flat = tuple(v for pair in reversed(pw) for v in pair)
    return torch.nn.functional.pad(x, flat)

kernel_name = 'gather_scatter__cheap_equivariant_egnn__graph'

def setup_inputs():

    _w = (lambda a: (lambda t: t.to(torch.bfloat16) if t.is_floating_point() else t)(torch.as_tensor(a)))
    N, deg, D, De = 50000, 16, 64, max(16, 1)
    E = N * deg
    return {
        "h": _w((lambda *s: torch.randn(*s, dtype=torch.bfloat16) * 0.2)(N, D)),
        "pos": _w((lambda *s: torch.randn(*s, dtype=torch.bfloat16) * 0.8)(N, 3)),
        "e_attr": _w((lambda *s: torch.randn(*s, dtype=torch.bfloat16) * 0.8)(E, De)),
        "src": _w((lambda lo, hi, *s: torch.randint(lo, hi, s))(0, N, E).to(torch.int64)),
        "dst": _w((lambda lo, hi, *s: torch.randint(lo, hi, s))(0, N, E).to(torch.int64)),
        "w_e": _w(0.1 * (lambda *s: torch.randn(*s, dtype=torch.bfloat16) * 0.8)(2 * D + 1 + De, D)),
        "w_x": _w(0.1 * (lambda *s: torch.randn(*s, dtype=torch.bfloat16) * 0.2)(D, 1)),
        "w_h": _w(0.1 * (lambda *s: torch.randn(*s, dtype=torch.bfloat16) * 0.2)(2 * D, D)),
    }

def reference(h, pos, e_attr, src, dst, w_e, w_x, w_h):
    N, D = h.shape[0], h.shape[1]
    diff = pos[dst] - pos[src]
    dist2 = torch.sum(diff * diff, axis=-1, keepdims=True)
    e_in = torch.concatenate([h[dst], h[src], dist2, e_attr], axis=-1)
    m_ij = _silu(e_in @ w_e)
    coord_scale = m_ij @ w_x
    pos_new = pos + _segment_sum(diff * coord_scale, dst, N)
    m_i = _segment_sum(m_ij, dst, N)
    h_new = h + _silu(torch.concatenate([h, m_i], axis=-1) @ w_h)
    return h_new, pos_new
```

```cpp
#include <hip/hip_runtime.h>
#include <hip/hip_bf16.h>
#include <stdint.h>

static __device__ __forceinline__ float bf2f(unsigned short v) { return __uint_as_float(((unsigned)v) << 16); }
static __device__ __forceinline__ unsigned short f2bf(float f) {
    __hip_bfloat16 b = __float2bfloat16(f); return *reinterpret_cast<unsigned short*>(&b);
}

__global__ __launch_bounds__(256) void edge_kernel(const unsigned short* h, const unsigned short* pos, const unsigned short* ea,
                            const int* src, const int* dst, const unsigned short* we, const unsigned short* wx,
                            float* macc, float* pacc, int E) {
    __shared__ float sw[145 * 64];
    for (int i = threadIdx.x; i < 145 * 64; i += blockDim.x) sw[i] = bf2f(we[i]);
    __syncthreads();
    int lane = threadIdx.x & 63;
    int wid = (blockIdx.x * blockDim.x + threadIdx.x) >> 6;
    int nw = (gridDim.x * blockDim.x) >> 6;
    float wxl = bf2f(wx[lane]);
    for (int e = wid; e < E; e += nw) {
        int s = src[e], d = dst[e];
        float dx = bf2f(f2bf(bf2f(pos[d * 3 + 0]) - bf2f(pos[s * 3 + 0])));
        float dy = bf2f(f2bf(bf2f(pos[d * 3 + 1]) - bf2f(pos[s * 3 + 1])));
        float dz = bf2f(f2bf(bf2f(pos[d * 3 + 2]) - bf2f(pos[s * 3 + 2])));
        float d2 = bf2f(f2bf(dx * dx + dy * dy + dz * dz));
        float hd = bf2f(h[d * 64 + lane]), hs = bf2f(h[s * 64 + lane]);
        float eav = lane < 16 ? bf2f(ea[(size_t)e * 16 + lane]) : 0.f;
        float acc = d2 * sw[128 * 64 + lane];
        _Pragma("unroll 8") for (int k = 0; k < 64; ++k) acc += __shfl(hd, k) * sw[k * 64 + lane];
        _Pragma("unroll 8") for (int k = 0; k < 64; ++k) acc += __shfl(hs, k) * sw[(64 + k) * 64 + lane];
        for (int k = 0; k < 16; ++k) acc += __shfl(eav, k) * sw[(129 + k) * 64 + lane];
        float m = acc / (1.f + __expf(-acc));
        float cs = m * wxl;
        for (int o = 32; o > 0; o >>= 1) cs += __shfl_xor(cs, o);
        atomicAdd(&macc[d * 64 + lane], m);
        if (lane < 3) atomicAdd(&pacc[d * 3 + lane], (lane == 0 ? dx : lane == 1 ? dy : dz) * cs);
    }
}

__global__ __launch_bounds__(256) void node_kernel(const unsigned short* h, const unsigned short* pos, const unsigned short* wh,
                            const float* macc, const float* pacc, unsigned short* out, int N) {
    __shared__ float sw[128 * 64];
    for (int i = threadIdx.x; i < 128 * 64; i += blockDim.x) sw[i] = bf2f(wh[i]);
    __syncthreads();
    int lane = threadIdx.x & 63;
    int wid = (blockIdx.x * blockDim.x + threadIdx.x) >> 6;
    int nw = (gridDim.x * blockDim.x) >> 6;
    for (int n = wid; n < N; n += nw) {
        float hv = bf2f(h[n * 64 + lane]);
        float mv = macc[n * 64 + lane];
        float acc = 0.f;
        _Pragma("unroll 8") for (int k = 0; k < 64; ++k) acc += __shfl(hv, k) * sw[k * 64 + lane];
        _Pragma("unroll 8") for (int k = 0; k < 64; ++k) acc += __shfl(mv, k) * sw[(64 + k) * 64 + lane];
        float s = acc / (1.f + __expf(-acc));
        out[n * 64 + lane] = f2bf(hv + s);
        if (lane < 3) out[(size_t)N * 64 + n * 3 + lane] = f2bf(bf2f(pos[n * 3 + lane]) + pacc[n * 3 + lane]);
    }
}

extern "C" void kernel_launch(void* const* d_in, const int* in_sizes, int n_in,
                              void* d_out, int out_size, void* d_ws, size_t ws_size,
                              hipStream_t stream) {
    const unsigned short* h = (const unsigned short*)d_in[0];
    const unsigned short* pos = (const unsigned short*)d_in[1];
    const unsigned short* ea = (const unsigned short*)d_in[2];
    const int* src = (const int*)d_in[3];
    const int* dst = (const int*)d_in[4];
    const unsigned short* we = (const unsigned short*)d_in[5];
    const unsigned short* wx = (const unsigned short*)d_in[6];
    const unsigned short* wh = (const unsigned short*)d_in[7];
    int N = in_sizes[0] / 64, E = in_sizes[3];
    float* macc = (float*)d_ws;
    float* pacc = macc + (size_t)N * 64;
    hipMemsetAsync(d_ws, 0, (size_t)N * 67 * 4, stream);
    edge_kernel<<<2048, 256, 0, stream>>>(h, pos, ea, src, dst, we, wx, macc, pacc, E);
    node_kernel<<<1024, 256, 0, stream>>>(h, pos, wh, macc, pacc, (unsigned short*)d_out, N);
}
```

```cpp
#include <hip/hip_runtime.h>
#include <hip/hip_bf16.h>
#include <stdint.h>

typedef unsigned short u16;
typedef unsigned int u32;
typedef u32 u32x4 __attribute__((ext_vector_type(4)));
typedef u32 u32x2 __attribute__((ext_vector_type(2)));
typedef float f32x4 __attribute__((ext_vector_type(4)));
typedef float f32x16 __attribute__((ext_vector_type(16)));
typedef short bf16x8 __attribute__((ext_vector_type(8)));
typedef short s16x4 __attribute__((ext_vector_type(4)));
#define LAS __attribute__((address_space(3)))
#define DI __device__ __forceinline__
#define MFMA(a, b, c) __builtin_amdgcn_mfma_f32_32x32x16_bf16((a), (b), (c), 0, 0, 0)

static DI float bf2f(u16 v) { return __uint_as_float(((u32)v) << 16); }
static DI u16 f2bf(float f) { __hip_bfloat16 b = __float2bfloat16(f); return *reinterpret_cast<u16*>(&b); }
static DI u32 cvtpk(float lo, float hi) { u32 r; asm("v_cvt_pk_bf16_f32 %0, %1, %2" : "=v"(r) : "v"(lo), "v"(hi)); return r; }

constexpr int N_ = 50000, E_ = 800000;
constexpr int WN = 25;
constexpr int NWIN = 2000;
constexpr int NCH = 256;
constexpr int CH = 3125;
constexpr int OFFP = 2048;
constexpr int CAP = 512;
constexpr int NFRAG = 40;
constexpr float NLOG2E = -1.4426950408889634f;
constexpr float NLN2 = -0.6931471805599453f;

constexpr size_t WS_OFFS = 0;
constexpr size_t WS_REC = WS_OFFS + (size_t)NCH * OFFP * 2;
constexpr size_t WS_EAS = WS_REC + (size_t)E_ * 4;
constexpr size_t WS_POS = WS_EAS + (size_t)E_ * 32;
constexpr size_t WS_WPK = WS_POS + (size_t)N_ * 16;
constexpr size_t WS_END = WS_WPK + (size_t)NFRAG * 1024;

constexpr int S_HIST = 0, S_WSUM = 8192, S_REC = 8192 + 128, S_EA = S_REC + 12544, S_BYTES = S_EA + CH * 32;

__global__ __launch_bounds__(1024) void sort_kernel(const int* __restrict__ src, const int* __restrict__ dst, const u32x4* __restrict__ ea,
                                                    const u16* __restrict__ pos, const u16* __restrict__ we, const u16* __restrict__ wx,
                                                    const u16* __restrict__ wh, char* __restrict__ ws) {
    extern __shared__ __attribute__((aligned(16))) char smem[];
    u32* hist = (u32*)(smem + S_HIST);
    u32* wsum = (u32*)(smem + S_WSUM);
    u32* srec = (u32*)(smem + S_REC);
    u32x4* sea = (u32x4*)(smem + S_EA);
    const int tid = threadIdx.x, lane = tid & 63, wv = tid >> 6, c = blockIdx.x;
    const int e0 = c * CH;
    int d[4], s[4]; u32x4 a0[4], a1[4];
#pragma unroll
    for (int k = 0; k < 4; ++k) { int i = tid + 1024 * k; i = i < CH ? i : CH - 1; d[k] = dst[e0 + i]; s[k] = src[e0 + i]; }
#pragma unroll
    for (int k = 0; k < 4; ++k) { int i = tid + 1024 * k; i = i < CH ? i : CH - 1; a0[k] = ea[(size_t)(e0 + i) * 2]; a1[k] = ea[(size_t)(e0 + i) * 2 + 1]; }
    hist[tid] = 0; hist[tid + 1024] = 0;

    {
        int n = c * 196 + tid;
        if (tid < 196 && n < N_) {
            f32x4 p = {bf2f(pos[n * 3]), bf2f(pos[n * 3 + 1]), bf2f(pos[n * 3 + 2]), 0.f};
            *(f32x4*)(ws + WS_POS + (size_t)n * 16) = p;
        }
        if (c < NFRAG && wv == 15) {
            const int r = lane & 31, h = lane >> 5;
            u16 v[8];
#pragma unroll
            for (int j = 0; j < 8; ++j) {
                const int kn = 8 * h + j;
                const int kp = 8 * (j >> 2) + 4 * h + (j & 3);
                u16 x = 0;
                if (c < 12) {
                    const int t = c / 6, st = c % 6, f = 32 * t + r;
                    if (st < 4) x = f2bf(NLOG2E * bf2f(we[(64 + 16 * st + kn) * 64 + f]));
                    else if (st == 4) x = f2bf(NLOG2E * bf2f(we[(129 + kn) * 64 + f]));
                    else {
                        const float wd = NLOG2E * bf2f(we[128 * 64 + f]);
                        const u16 hi = f2bf(wd);
                        x = kn == 0 ? hi : (kn == 1 ? f2bf(wd - bf2f(hi)) : (u16)0);
                    }
                } else if (c < 20) {
                    const int t = (c - 12) >> 2, st = (c - 12) & 3;
                    x = we[(16 * st + kn) * 64 + 32 * t + r];
                } else if (c < 24) {
                    const int t = (c - 20) >> 1, st = (c - 20) & 1;
                    x = (r == 0 || r == 4) ? wx[32 * t + 16 * st + kp] : (u16)0;
                } else if (c < 32) {
                    const int t = (c - 24) >> 2, st = (c - 24) & 3;
                    x = wh[(16 * st + kn) * 64 + 32 * t + r];
                } else {
                    const int t = (c - 32) >> 2, st = (c - 32) & 3;
                    x = wh[(64 + 16 * st + kp) * 64 + 32 * t + r];
                }
                v[j] = x;
            }
            u32x4 o = {v[0] | ((u32)v[1] << 16), v[2] | ((u32)v[3] << 16), v[4] | ((u32)v[5] << 16), v[6] | ((u32)v[7] << 16)};
            *(u32x4*)(ws + WS_WPK + (size_t)c * 1024 + lane * 16) = o;
        }
    }
    __syncthreads();
    int wn[4]; u32 rk[4];
#pragma unroll
    for (int k = 0; k < 4; ++k) {
        wn[k] = (u32)d[k] / (u32)WN;
        if (tid + 1024 * k < CH) rk[k] = atomicAdd(&hist[wn[k]], 1u);
    }
    __syncthreads();
    const u32 h0 = hist[2 * tid], h1 = hist[2 * tid + 1];
    u32 x = h0 + h1;
#pragma unroll
    for (int o = 1; o < 64; o <<= 1) { u32 y = __shfl_up(x, o); if (lane >= o) x += y; }
    if (lane == 63) wsum[wv] = x;
    __syncthreads();
    u32 pre = 0;
#pragma unroll
    for (int i = 0; i < 16; ++i) pre += i < wv ? wsum[i] : 0u;
    const u32 st0 = pre + x - h0 - h1, st1 = st0 + h0;
    hist[2 * tid] = st0; hist[2 * tid + 1] = st1;
    *(u32*)(ws + WS_OFFS + ((size_t)c * OFFP + 2 * tid) * 2) = st0 | (st1 << 16);
    __syncthreads();
#pragma unroll
    for (int k = 0; k < 4; ++k) {
        if (tid + 1024 * k < CH) {
            const u32 p = hist[wn[k]] + rk[k];
            srec[p] = (u32)s[k] | ((u32)(d[k] - wn[k] * WN) << 16);
            sea[2 * p] = a0[k]; sea[2 * p + 1] = a1[k];
        }
    }
    __syncthreads();
    u32x4* gea = (u32x4*)(ws + WS_EAS) + (size_t)e0 * 2;
    u32* grec = (u32*)(ws + WS_REC) + e0;
#pragma unroll
    for (int k = 0; k < 7; ++k) { int i = tid + 1024 * k; if (i < 2 * CH) gea[i] = sea[i]; }
#pragma unroll
    for (int k = 0; k < 4; ++k) { int i = tid + 1024 * k; if (i < CH) grec[i] = srec[i]; }
}

constexpr int L_H = 0, L_MS = 8192, L_AN = 10240, L_IDX = 17040, L_SRC = 19088, L_PA = 20112, L_WAVE = 20480;
constexpr int ANP = 272;

static DI void dma16(u32 voff, __amdgpu_buffer_rsrc_t rs, u32 ldsaddr) {
    u32 keep;
    asm volatile("s_mov_b32 %0, m0\n\ts_mov_b32 m0, %3\n\ts_nop 0\n\tbuffer_load_dwordx4 %1, %2, 0 offen lds\n\ts_mov_b32 m0, %0"
                 : "=&s"(keep) : "v"(voff), "s"(rs), "s"(ldsaddr) : "memory");
}
static DI u32x4 bload16(u32 voff, __amdgpu_buffer_rsrc_t rs) {
    u32x4 r; asm volatile("buffer_load_dwordx4 %0, %1, %2, 0 offen" : "=v"(r) : "v"(voff), "s"(rs) : "memory"); return r;
}
static DI s16x4 trread(LAS const char* p) {
    typedef short v4i16_t __attribute__((ext_vector_type(4)));
    return __builtin_bit_cast(s16x4, __builtin_amdgcn_ds_read_tr16_b64_v4i16((LAS v4i16_t*)p));
}
#define CBAR() asm volatile("" ::: "memory")

__global__ __launch_bounds__(512, 2) void edge_kernel(const u16* __restrict__ hg, const u16* __restrict__ posg, const char* __restrict__ ws,
                                                      u16* __restrict__ out) {
    extern __shared__ __attribute__((aligned(16))) char smem[];
    const int lane = threadIdx.x & 63, e = lane & 31, hh = lane >> 5;
    const int wv = __builtin_amdgcn_readfirstlane(threadIdx.x >> 6);
    const int w = blockIdx.x * 8 + wv, nbase = w * WN;
    LAS char* L = (LAS char*)smem + wv * L_WAVE;
    const u32 lbase = (u32)(uintptr_t)L;
    LAS u32* lidx = (LAS u32*)(L + L_IDX);
    LAS u16* lsrc = (LAS u16*)(L + L_SRC);
    LAS float* pacc = (LAS float*)(L + L_PA);

    const u16* offs = (const u16*)(ws + WS_OFFS);
    const u32* rec = (const u32*)(ws + WS_REC);
    const u32x4* wpk = (const u32x4*)(ws + WS_WPK);
    const f32x4* pos4 = (const f32x4*)(ws + WS_POS);
    const auto rs_h = __builtin_amdgcn_make_buffer_rsrc((void*)hg, 0, N_ * 128, 0x00020000);
    const auto rs_e = __builtin_amdgcn_make_buffer_rsrc((void*)(ws + WS_EAS), 0, E_ * 32, 0x00020000);
    const auto rs_p = __builtin_amdgcn_make_buffer_rsrc((void*)(ws + WS_POS), 0, N_ * 16, 0x00020000);

    u32 cs_[4], cn_[4];
#pragma unroll
    for (int q = 0; q < 4; ++q) {
        const u16* p = offs + (size_t)(lane * 4 + q) * OFFP + w;
        const u32 a = p[0], b = p[1];
        cs_[q] = a; cn_[q] = b - a;
    }
    const int nn = nbase + (e < WN ? e : WN - 1);
    bf16x8 hw[4];
#pragma unroll
    for (int s = 0; s < 4; ++s) hw[s] = *(const bf16x8*)(hg + (size_t)nn * 64 + 16 * s + 8 * hh);
    const f32x4 pown = pos4[nn];
    {
        f32x16 an0 = {0}, an1 = {0};
#pragma unroll
        for (int s = 0; s < 4; ++s) {
            an0 = MFMA(__builtin_bit_cast(bf16x8, wpk[(12 + s) * 64 + lane]), hw[s], an0);
            an1 = MFMA(__builtin_bit_cast(bf16x8, wpk[(16 + s) * 64 + lane]), hw[s], an1);
        }
        if (e < WN) {
#pragma unroll
            for (int g = 0; g < 4; ++g) {
                f32x4 v0 = {an0[4 * g] * NLOG2E, an0[4 * g + 1] * NLOG2E, an0[4 * g + 2] * NLOG2E, an0[4 * g + 3] * NLOG2E};
                f32x4 v1 = {an1[4 * g] * NLOG2E, an1[4 * g + 1] * NLOG2E, an1[4 * g + 2] * NLOG2E, an1[4 * g + 3] * NLOG2E};
                *(LAS f32x4*)(L + L_AN + e * ANP + 32 * g + 16 * hh) = v0;
                *(LAS f32x4*)(L + L_AN + e * ANP + 128 + 32 * g + 16 * hh) = v1;
            }
        }
        pacc[lane] = 0.f; if (lane < 16) pacc[64 + lane] = 0.f;
    }
    bf16x8 wf[12], wxf[4];
#pragma unroll
    for (int i = 0; i < 12; ++i) wf[i] = __builtin_bit_cast(bf16x8, wpk[i * 64 + lane]);
#pragma unroll
    for (int i = 0; i < 4; ++i) wxf[i] = __builtin_bit_cast(bf16x8, wpk[(20 + i) * 64 + lane]);

    const u32 ltot = cn_[0] + cn_[1] + cn_[2] + cn_[3];
    u32 inc = ltot;
#pragma unroll
    for (int o = 1; o < 64; o <<= 1) { u32 y = __shfl_up(inc, o); if (lane >= o) inc += y; }
    const int T = __builtin_amdgcn_readlane((int)inc, 63);
    u32 lstart = inc - ltot;

#pragma unroll
    for (int i = 0; i < 12; ++i) asm volatile("" : "+v"(wf[i]));
#pragma unroll
    for (int i = 0; i < 4; ++i) asm volatile("" : "+v"(wxf[i]));

    const u32 fsw = (e >> 1) & 7;
    LAS const char* hrd[4];
#pragma unroll
    for (int s = 0; s < 4; ++s) hrd[s] = L + L_H + e * 128 + (((2 * s + hh) ^ fsw) << 4);
    const u32 swz0 = ((lane & 7) ^ (lane >> 4)) << 4, swz1 = swz0 ^ 64;
    LAS char* mwr = L + L_MS + e * 64;
    const int i16 = lane & 15, tq = i16 >> 2, tp = i16 & 3, tb = (lane >> 4) & 1;
    LAS const char* mrd[4];
#pragma unroll
    for (int i = 0; i < 4; ++i) {
        const int row = 16 * (i >> 1) + 8 * hh + 4 * (i & 1) + tq;
        mrd[i] = L + L_MS + row * 64 + (((4 * tb + tp) ^ ((row >> 1) & 7)) << 3);
    }
    LAS const char* srd[2];
#pragma unroll
    for (int s = 0; s < 2; ++s) srd[s] = L + L_MS + e * 64 + (((2 * s + hh) ^ ((e >> 2) & 3)) << 4);

    f32x16 y0 = {0}, y1 = {0};

    for (int b0 = 0; b0 < T; b0 += CAP) {
        const int nb = (T - b0) < CAP ? (T - b0) : CAP;
        const int nt = (nb + 31) >> 5;
        {
            int ln = lane; asm volatile("" : "+v"(ln));
            u32 mx = max(max(cn_[0], cn_[1]), max(cn_[2], cn_[3]));
#pragma unroll
            for (int o = 32; o > 0; o >>= 1) mx = max(mx, (u32)__shfl_xor((int)mx, o));
            const u32 maxc = __builtin_amdgcn_readfirstlane(mx);
            const u32 p0 = lstart - (u32)b0, p1 = p0 + cn_[0], p2 = p1 + cn_[1], p3 = p2 + cn_[2];
            const u32 g0 = (u32)(ln * 4) * CH + cs_[0], g1 = (u32)(ln * 4 + 1) * CH + cs_[1], g2 = (u32)(ln * 4 + 2) * CH + cs_[2], g3 = (u32)(ln * 4 + 3) * CH + cs_[3];
#pragma clang loop unroll(disable) vectorize(disable)
            for (u32 j = 0; j < maxc; ++j) {
                if (j < cn_[0] && p0 + j < (u32)CAP) lidx[p0 + j] = g0 + j;
                if (j < cn_[1] && p1 + j < (u32)CAP) lidx[p1 + j] = g1 + j;
                if (j < cn_[2] && p2 + j < (u32)CAP) lidx[p2 + j] = g2 + j;
                if (j < cn_[3] && p3 + j < (u32)CAP) lidx[p3 + j] = g3 + j;
            }
            CBAR();
            u32 gi[8], rr[8];
#pragma unroll
            for (int k = 0; k < 8; ++k) { const int i = ln + 64 * k; gi[k] = i < nb ? lidx[i] : 0u; }
#pragma unroll
            for (int k = 0; k < 8; ++k) { const int i = ln + 64 * k; rr[k] = i < nb ? rec[gi[k]] : ((u32)WN << 16); }
#pragma unroll
            for (int k = 0; k < 8; ++k) { const int i = ln + 64 * k; lidx[i] = gi[k] | ((rr[k] >> 16) << 20); lsrc[i] = (u16)rr[k]; }
        }
        CBAR();

        u32x4 eaA, eaB, psA, psB;
#define ISSUE(TT, BUF, EA, PS) do { \
            const int tb_ = (TT) * 32; \
            const u32 s0_ = lsrc[tb_ + (lane >> 3)], s1_ = lsrc[tb_ + 8 + (lane >> 3)], s2_ = lsrc[tb_ + 16 + (lane >> 3)], s3_ = lsrc[tb_ + 24 + (lane >> 3)]; \
            const u32 li_ = lidx[tb_ + e], so_ = lsrc[tb_ + e]; \
            dma16(s0_ * 128 + swz0, rs_h, lbase + L_H + (BUF) * 4096); \
            dma16(s1_ * 128 + swz1, rs_h, lbase + L_H + (BUF) * 4096 + 1024); \
            dma16(s2_ * 128 + swz0, rs_h, lbase + L_H + (BUF) * 4096 + 2048); \
            dma16(s3_ * 128 + swz1, rs_h, lbase + L_H + (BUF) * 4096 + 3072); \
            EA = bload16((li_ & 0xFFFFFu) * 32 + hh * 16, rs_e); \
            PS = bload16(so_ * 16, rs_p); \
        } while (0)

#define COMPUTE(TT, BUF, EA, PS) do { \
            if ((TT) + 1 < nt) asm volatile("s_waitcnt vmcnt(6)" : "+v"(EA), "+v"(PS) :: "memory"); \
            else asm volatile("s_waitcnt vmcnt(0)" : "+v"(EA), "+v"(PS) :: "memory"); \
            const u32 dl = lidx[(TT) * 32 + e] >> 20; \
            bf16x8 xb[4]; \
            _Pragma("unroll") for (int s = 0; s < 4; ++s) xb[s] = *(LAS const bf16x8*)(hrd[s] + (BUF) * 4096); \
            const u32 dla = dl < WN ? dl : WN - 1; \
            LAS const char* anp = L + L_AN + dla * ANP + 16 * hh; \
            f32x16 c0, c1; \
            _Pragma("unroll") for (int g = 0; g < 4; ++g) { \
                const f32x4 v0 = *(LAS const f32x4*)(anp + 32 * g), v1 = *(LAS const f32x4*)(anp + 128 + 32 * g); \
                c0[4 * g] = v0[0]; c0[4 * g + 1] = v0[1]; c0[4 * g + 2] = v0[2]; c0[4 * g + 3] = v0[3]; \
                c1[4 * g] = v1[0]; c1[4 * g + 1] = v1[1]; c1[4 * g + 2] = v1[2]; c1[4 * g + 3] = v1[3]; } \
              \
            CBAR(); \
            *(LAS u32x4*)(L + L_MS + lane * 16) = (u32x4){0, 0, 0, 0}; *(LAS u32x4*)(L + L_MS + 1024 + lane * 16) = (u32x4){0, 0, 0, 0}; \
            CBAR(); \
            *(LAS u16*)(L + L_MS + dl * 64 + ((((u32)e >> 3) ^ ((dl >> 2) & 3)) << 4) + (e & 7) * 2) = (u16)0x3F80; \
            CBAR(); \
            const bf16x8 sf0 = *(LAS const bf16x8*)srd[0], sf1 = *(LAS const bf16x8*)srd[1]; \
            CBAR(); \
            const bf16x8 xe = __builtin_bit_cast(bf16x8, EA); \
            const float dx = __shfl(pown[0], dla) - __uint_as_float(PS[0]); \
            const float dy = __shfl(pown[1], dla) - __uint_as_float(PS[1]); \
            const float dz = __shfl(pown[2], dla) - __uint_as_float(PS[2]); \
            const float d2 = dx * dx + dy * dy + dz * dz; \
            const u32x4 dfr = {cvtpk(d2, d2), 0u, 0u, 0u}; \
            const bf16x8 xd = __builtin_bit_cast(bf16x8, dfr); \
            c0 = MFMA(wf[4], xe, c0); c1 = MFMA(wf[10], xe, c1); \
            asm volatile("s_waitcnt lgkmcnt(0)" : "+v"(xb[0]), "+v"(xb[1]), "+v"(xb[2]), "+v"(xb[3]), "+v"(c0), "+v"(c1) :: "memory"); \
            if ((TT) + 2 < nt) ISSUE((TT) + 2, BUF, EA, PS); \
            _Pragma("unroll") for (int s = 0; s < 4; ++s) { c0 = MFMA(wf[s], xb[s], c0); c1 = MFMA(wf[6 + s], xb[s], c1); } \
            c0 = MFMA(wf[5], xd, c0); c1 = MFMA(wf[11], xd, c1); \
            u32 pk0[8], pk1[8]; \
            _Pragma("unroll") for (int i = 0; i < 8; ++i) { \
                const float a0 = c0[2 * i], a1 = c0[2 * i + 1], b0_ = c1[2 * i], b1_ = c1[2 * i + 1]; \
                pk0[i] = cvtpk(a0 * __builtin_amdgcn_rcpf(1.f + __builtin_amdgcn_exp2f(a0)), a1 * __builtin_amdgcn_rcpf(1.f + __builtin_amdgcn_exp2f(a1))); \
                pk1[i] = cvtpk(b0_ * __builtin_amdgcn_rcpf(1.f + __builtin_amdgcn_exp2f(b0_)), b1_ * __builtin_amdgcn_rcpf(1.f + __builtin_amdgcn_exp2f(b1_))); } \
            f32x16 cz = {0}; \
            cz = MFMA(wxf[0], __builtin_bit_cast(bf16x8, (u32x4){pk0[0], pk0[1], pk0[2], pk0[3]}), cz); \
            cz = MFMA(wxf[1], __builtin_bit_cast(bf16x8, (u32x4){pk0[4], pk0[5], pk0[6], pk0[7]}), cz); \
            cz = MFMA(wxf[2], __builtin_bit_cast(bf16x8, (u32x4){pk1[0], pk1[1], pk1[2], pk1[3]}), cz); \
            cz = MFMA(wxf[3], __builtin_bit_cast(bf16x8, (u32x4){pk1[4], pk1[5], pk1[6], pk1[7]}), cz); \
            CBAR(); \
            _Pragma("unroll") for (int g = 0; g < 4; ++g) *(LAS u32x2*)(mwr + (((2 * g + hh) ^ fsw) << 3)) = (u32x2){pk0[2 * g], pk0[2 * g + 1]}; \
            CBAR(); \
            { const s16x4 t0 = trread(mrd[0]), t1 = trread(mrd[1]), t2 = trread(mrd[2]), t3 = trread(mrd[3]); \
              CBAR(); \
              _Pragma("unroll") for (int g = 0; g < 4; ++g) *(LAS u32x2*)(mwr + (((2 * g + hh) ^ fsw) << 3)) = (u32x2){pk1[2 * g], pk1[2 * g + 1]}; \
              CBAR(); \
              const s16x4 u0 = trread(mrd[0]), u1 = trread(mrd[1]), u2 = trread(mrd[2]), u3 = trread(mrd[3]); \
              CBAR(); \
              y0 = MFMA(__builtin_shufflevector(t0, t1, 0, 1, 2, 3, 4, 5, 6, 7), sf0, y0); \
              y0 = MFMA(__builtin_shufflevector(t2, t3, 0, 1, 2, 3, 4, 5, 6, 7), sf1, y0); \
              y1 = MFMA(__builtin_shufflevector(u0, u1, 0, 1, 2, 3, 4, 5, 6, 7), sf0, y1); \
              y1 = MFMA(__builtin_shufflevector(u2, u3, 0, 1, 2, 3, 4, 5, 6, 7), sf1, y1); } \
            const float csv = cz[0] * NLN2; \
            __hip_atomic_fetch_add(pacc + dl * 3 + hh, csv * (hh ? dy : dx), __ATOMIC_RELAXED, __HIP_MEMORY_SCOPE_WORKGROUP); \
            if (hh == 0) __hip_atomic_fetch_add(pacc + dl * 3 + 2, csv * dz, __ATOMIC_RELAXED, __HIP_MEMORY_SCOPE_WORKGROUP); \
        } while (0)

        ISSUE(0, 0, eaA, psA);
        if (nt > 1) ISSUE(1, 1, eaB, psB);
        for (int t = 0; t < nt; t += 2) {
            COMPUTE(t, 0, eaA, psA);
            if (t + 1 >= nt) break;
            COMPUTE(t + 1, 1, eaB, psB);
        }
        CBAR();
        if (b0 + CAP < T) {
            u32 acc_ = 0;
            int ln = lane; asm volatile("" : "+v"(ln));
#pragma unroll
            for (int q = 0; q < 4; ++q) {
                const u16* p = offs + (size_t)(ln * 4 + q) * OFFP + w;
                const u32 a = p[0], b = p[1];
                cs_[q] = a; cn_[q] = b - a; acc_ += b - a;
            }
            u32 inc2 = acc_;
#pragma unroll
            for (int o = 1; o < 64; o <<= 1) { u32 y = __shfl_up(inc2, o); if (lane >= o) inc2 += y; }
            lstart = inc2 - acc_;
        } else {
#pragma unroll
            for (int q = 0; q < 4; ++q) { cs_[q] = 0; cn_[q] = 0; }
            lstart = 0;
        }
    }

    {
        int lane = threadIdx.x & 63; asm volatile("" : "+v"(lane) :: "memory");
        const int e = lane & 31, hh = lane >> 5;
        const int nn = nbase + (e < WN ? e : WN - 1);
#pragma unroll
        for (int s = 0; s < 4; ++s) hw[s] = *(const bf16x8*)(hg + (size_t)nn * 64 + 16 * s + 8 * hh);
        u32 p0[8], p1[8];
#pragma unroll
        for (int i = 0; i < 8; ++i) { p0[i] = cvtpk(y0[2 * i] * NLN2, y0[2 * i + 1] * NLN2); p1[i] = cvtpk(y1[2 * i] * NLN2, y1[2 * i + 1] * NLN2); }
        bf16x8 mb[4];
        mb[0] = __builtin_bit_cast(bf16x8, (u32x4){p0[0], p0[1], p0[2], p0[3]});
        mb[1] = __builtin_bit_cast(bf16x8, (u32x4){p0[4], p0[5], p0[6], p0[7]});
        mb[2] = __builtin_bit_cast(bf16x8, (u32x4){p1[0], p1[1], p1[2], p1[3]});
        mb[3] = __builtin_bit_cast(bf16x8, (u32x4){p1[4], p1[5], p1[6], p1[7]});
        f32x16 o0 = {0}, o1 = {0};
#pragma unroll
        for (int s = 0; s < 4; ++s) {
            o0 = MFMA(__builtin_bit_cast(bf16x8, wpk[(24 + s) * 64 + lane]), hw[s], o0);
            o1 = MFMA(__builtin_bit_cast(bf16x8, wpk[(28 + s) * 64 + lane]), hw[s], o1);
            o0 = MFMA(__builtin_bit_cast(bf16x8, wpk[(32 + s) * 64 + lane]), mb[s], o0);
            o1 = MFMA(__builtin_bit_cast(bf16x8, wpk[(36 + s) * 64 + lane]), mb[s], o1);
        }
        if (e < WN) {
#pragma unroll
            for (int g = 0; g < 8; ++g) {
                const int f = 32 * (g >> 2) + 8 * (g & 3) + 4 * hh;
                const u32x2 hv = *(const u32x2*)(hg + (size_t)nn * 64 + f);
                float r[4];
#pragma unroll
                for (int i = 0; i < 4; ++i) {
                    const float a = g < 4 ? o0[4 * (g & 3) + i] : o1[4 * (g & 3) + i];
                    const float sv = a * __builtin_amdgcn_rcpf(1.f + __builtin_amdgcn_exp2f(a * NLOG2E));
                    const u32 hw_ = hv[i >> 1];
                    const float hf = __uint_as_float((i & 1) ? (hw_ & 0xFFFF0000u) : (hw_ << 16));
                    r[i] = hf + sv;
                }
                *(u32x2*)(out + (size_t)nn * 64 + f) = (u32x2){cvtpk(r[0], r[1]), cvtpk(r[2], r[3])};
            }
        }
        CBAR();
        for (int i = lane; i < WN * 3; i += 64) {
            const size_t o = (size_t)nbase * 3 + i;
            out[(size_t)N_ * 64 + o] = f2bf(bf2f(posg[o]) + pacc[i]);
        }
    }
}

__global__ __launch_bounds__(256) void g_edge_kernel(const u16* h, const u16* pos, const u16* ea, const int* src, const int* dst,
                                                     const u16* we, const u16* wx, float* macc, float* pacc, int E) {
    __shared__ float sw[145 * 64];
    for (int i = threadIdx.x; i < 145 * 64; i += blockDim.x) sw[i] = bf2f(we[i]);
    __syncthreads();
    int lane = threadIdx.x & 63;
    int wid = (blockIdx.x * blockDim.x + threadIdx.x) >> 6;
    int nw = (gridDim.x * blockDim.x) >> 6;
    float wxl = bf2f(wx[lane]);
    for (int e = wid; e < E; e += nw) {
        int s = src[e], d = dst[e];
        float dx = bf2f(pos[(size_t)d * 3 + 0]) - bf2f(pos[(size_t)s * 3 + 0]);
        float dy = bf2f(pos[(size_t)d * 3 + 1]) - bf2f(pos[(size_t)s * 3 + 1]);
        float dz = bf2f(pos[(size_t)d * 3 + 2]) - bf2f(pos[(size_t)s * 3 + 2]);
        float d2 = dx * dx + dy * dy + dz * dz;
        float hd = bf2f(h[(size_t)d * 64 + lane]), hs = bf2f(h[(size_t)s * 64 + lane]);
        float eav = lane < 16 ? bf2f(ea[(size_t)e * 16 + lane]) : 0.f;
        float acc = d2 * sw[128 * 64 + lane];
#pragma unroll 8
        for (int k = 0; k < 64; ++k) acc += __shfl(hd, k) * sw[k * 64 + lane];
#pragma unroll 8
        for (int k = 0; k < 64; ++k) acc += __shfl(hs, k) * sw[(64 + k) * 64 + lane];
#pragma unroll 8
        for (int k = 0; k < 16; ++k) acc += __shfl(eav, k) * sw[(129 + k) * 64 + lane];
        float m = acc / (1.f + __expf(-acc));
        float cs = m * wxl;
        for (int o = 32; o > 0; o >>= 1) cs += __shfl_xor(cs, o);
        atomicAdd(&macc[(size_t)d * 64 + lane], m);
        if (lane < 3) atomicAdd(&pacc[(size_t)d * 3 + lane], (lane == 0 ? dx : lane == 1 ? dy : dz) * cs);
    }
}
__global__ __launch_bounds__(256) void g_node_kernel(const u16* h, const u16* pos, const u16* wh, const float* macc, const float* pacc,
                                                     u16* out, int N) {
    __shared__ float sw[128 * 64];
    for (int i = threadIdx.x; i < 128 * 64; i += blockDim.x) sw[i] = bf2f(wh[i]);
    __syncthreads();
    int lane = threadIdx.x & 63;
    int wid = (blockIdx.x * blockDim.x + threadIdx.x) >> 6;
    int nw = (gridDim.x * blockDim.x) >> 6;
    for (int n = wid; n < N; n += nw) {
        float hv = bf2f(h[(size_t)n * 64 + lane]);
        float mv = macc[(size_t)n * 64 + lane];
        float acc = 0.f;
#pragma unroll 8
        for (int k = 0; k < 64; ++k) acc += __shfl(hv, k) * sw[k * 64 + lane];
#pragma unroll 8
        for (int k = 0; k < 64; ++k) acc += __shfl(mv, k) * sw[(64 + k) * 64 + lane];
        float s = acc / (1.f + __expf(-acc));
        out[(size_t)n * 64 + lane] = f2bf(hv + s);
        if (lane < 3) out[(size_t)N * 64 + (size_t)n * 3 + lane] = f2bf(bf2f(pos[(size_t)n * 3 + lane]) + pacc[(size_t)n * 3 + lane]);
    }
}

extern "C" void kernel_launch(void* const* d_in, const int* in_sizes, int n_in,
                              void* d_out, int out_size, void* d_ws, size_t ws_size,
                              hipStream_t stream) {
    const u16* h = (const u16*)d_in[0];
    const u16* pos = (const u16*)d_in[1];
    const u16* ea = (const u16*)d_in[2];
    const int* src = (const int*)d_in[3];
    const int* dst = (const int*)d_in[4];
    const u16* we = (const u16*)d_in[5];
    const u16* wx = (const u16*)d_in[6];
    const u16* wh = (const u16*)d_in[7];
    const int N = in_sizes[0] / 64, E = in_sizes[3];
    if (N == N_ && E == E_ && ws_size >= WS_END) {
        (void)hipFuncSetAttribute((const void*)sort_kernel, hipFuncAttributeMaxDynamicSharedMemorySize, S_BYTES);
        (void)hipFuncSetAttribute((const void*)edge_kernel, hipFuncAttributeMaxDynamicSharedMemorySize, 8 * L_WAVE);
        sort_kernel<<<NCH, 1024, S_BYTES, stream>>>(src, dst, (const u32x4*)ea, pos, we, wx, wh, (char*)d_ws);
        edge_kernel<<<NWIN / 8, 512, 8 * L_WAVE, stream>>>(h, pos, (const char*)d_ws, (u16*)d_out);
    } else {
        float* macc = (float*)d_ws;
        float* pacc = macc + (size_t)N * 64;
        (void)hipMemsetAsync(d_ws, 0, (size_t)N * 67 * 4, stream);
        g_edge_kernel<<<2048, 256, 0, stream>>>(h, pos, ea, src, dst, we, wx, macc, pacc, E);
        g_node_kernel<<<1024, 256, 0, stream>>>(h, pos, wh, macc, pacc, (u16*)d_out, N);
    }
}
```

```cpp
#include <hip/hip_runtime.h>
#include <hip/hip_bf16.h>
#include <stdint.h>

#define PRIO 1
#define PBIAS 96
#define EANT 0
#define ABL 0
typedef unsigned short u16;
typedef unsigned int u32;
typedef u32 u32x4 __attribute__((ext_vector_type(4)));
typedef u32 u32x2 __attribute__((ext_vector_type(2)));
typedef float f32x4 __attribute__((ext_vector_type(4)));
typedef float f32x16 __attribute__((ext_vector_type(16)));
typedef short bf16x8 __attribute__((ext_vector_type(8)));
typedef short s16x4 __attribute__((ext_vector_type(4)));
#define LAS __attribute__((address_space(3)))
#define DI __device__ __forceinline__
static __device__ __forceinline__ f32x16 mfma_w(bf16x8 a, bf16x8 b, f32x16 c) {
    if (ABL & 64) { asm volatile("" : "+v"(c) : "v"(a), "v"(b)); return c; }
    return __builtin_amdgcn_mfma_f32_32x32x16_bf16(a, b, c, 0, 0, 0); }
#define MFMA(a, b, c) mfma_w((a), (b), (c))

static DI float bf2f(u16 v) { return __uint_as_float(((u32)v) << 16); }
static DI u16 f2bf(float f) { __hip_bfloat16 b = __float2bfloat16(f); return *reinterpret_cast<u16*>(&b); }
typedef float f32x2 __attribute__((ext_vector_type(2)));
typedef __bf16 bf16x2 __attribute__((ext_vector_type(2)));
static DI u32 cvtpk(float lo, float hi) { return __builtin_bit_cast(u32, __builtin_convertvector((f32x2){lo, hi}, bf16x2)); }

static DI u32 wave_scan(u32 x) {
    x += __builtin_amdgcn_update_dpp(0u, x, 0x111, 0xf, 0xf, false);
    x += __builtin_amdgcn_update_dpp(0u, x, 0x112, 0xf, 0xf, false);
    x += __builtin_amdgcn_update_dpp(0u, x, 0x114, 0xf, 0xf, false);
    x += __builtin_amdgcn_update_dpp(0u, x, 0x118, 0xf, 0xf, false);
    x += __builtin_amdgcn_update_dpp(0u, x, 0x142, 0xa, 0xf, false);
    x += __builtin_amdgcn_update_dpp(0u, x, 0x143, 0xc, 0xf, false);
    return x;
}

constexpr int N_ = 50000, E_ = 800000;
constexpr int WN = 25;
constexpr int NWIN = 2000;
constexpr int NBK = NWIN / 8;
constexpr int NCH = 256;
constexpr int CH = 3125;
constexpr int INL = 27;
constexpr int CAP = 512;
constexpr int NFRAG = 39;
constexpr float NLOG2E = -1.4426950408889634f;
constexpr float NLN2 = -0.6931471805599453f;

constexpr size_t WS_BLK = 0;
constexpr size_t WS_REC = WS_BLK + (size_t)NBK * NCH * 128;
constexpr size_t WS_POS = WS_REC + (size_t)E_ * 4;
constexpr size_t WS_WPK = WS_POS + (size_t)N_ * 16;
constexpr size_t WS_END = WS_WPK + (size_t)NFRAG * 1024;

constexpr int S_HIST = 0, S_WSUM = 8192, S_BLK = 8192 + 128, S_BYTES = S_BLK + NBK * 128;

#define SST(i)
#define SST_DECL
#define SST_FLUSH()
__global__ __launch_bounds__(1024) void sort_kernel(const int* __restrict__ src, const int* __restrict__ dst,
                                                    const u16* __restrict__ pos, const u16* __restrict__ we, const u16* __restrict__ wx,
                                                    const u16* __restrict__ wh, char* __restrict__ ws) {
    extern __shared__ __attribute__((aligned(16))) char smem[];
    u32* hist = (u32*)(smem + S_HIST);
    u32* wsum = (u32*)(smem + S_WSUM);
    u32* blk = (u32*)(smem + S_BLK);
    const int tid = threadIdx.x, lane = tid & 63, wv = tid >> 6, c = blockIdx.x;
    const int e0 = c * CH;
    SST_DECL SST(0);
    int d[4], s[4];
#pragma unroll
    for (int k = 0; k < 4; ++k) { int i = tid + 1024 * k; i = i < CH ? i : CH - 1; d[k] = dst[e0 + i]; s[k] = src[e0 + i]; }
    hist[tid] = 0; hist[tid + 1024] = 0;

    {
        int n = c * 196 + tid;
        if (tid < 196 && n < N_) {
            f32x4 p = {bf2f(pos[n * 3]), bf2f(pos[n * 3 + 1]), bf2f(pos[n * 3 + 2]), 0.f};
            *(f32x4*)(ws + WS_POS + (size_t)n * 16) = p;
        }
        if (c < NFRAG && wv == 15) {
            const int r = lane & 31, h = lane >> 5;
            const u16* sp; int base, perm = 0, mode = 0;
            if (c < 10) { const int t = c / 5, st = c % 5; sp = we; base = (st < 4 ? 64 + 16 * st : 129) * 64 + 32 * t; mode = 1; }
            else if (c < 18) { sp = we; base = 16 * ((c - 10) & 3) * 64 + 32 * ((c - 10) >> 2); }
            else if (c < 26) { sp = wh; base = 16 * ((c - 18) & 3) * 64 + 32 * ((c - 18) >> 2); }
            else if (c < 34) { sp = wh; base = (64 + 16 * ((c - 26) & 3)) * 64 + 32 * ((c - 26) >> 2); perm = 1; }
            else if (c == 34) { sp = we; base = 128 * 64; mode = 2; }
            else { sp = wx; base = 16 * (c - 35); perm = 1; mode = 3; }
            u16 v[8];
#pragma unroll
            for (int j = 0; j < 8; ++j) {
                const int k = perm ? 8 * (j >> 2) + 4 * h + (j & 3) : 8 * h + j;
                const int idx = mode == 2 ? 32 * ((j >> 1) & 1) + r : (mode == 3 ? k : k * 64 + r);
                v[j] = sp[base + idx];
            }
            const bool on = ((lane & 15) & 7) == 4 * ((lane >> 4) & 1);
#pragma unroll
            for (int j = 0; j < 8; ++j) {
                const float sc = NLOG2E * bf2f(v[j]);
                const u16 hi = f2bf(sc);
                const u16 lo = f2bf(sc - bf2f(hi));
                v[j] = mode == 0 ? v[j] : mode == 1 ? hi : mode == 2 ? ((j & 1) ? lo : hi) : (on ? v[j] : (u16)0);
            }
            u32x4 o = {v[0] | ((u32)v[1] << 16), v[2] | ((u32)v[3] << 16), v[4] | ((u32)v[5] << 16), v[6] | ((u32)v[7] << 16)};
            *(u32x4*)(ws + WS_WPK + (size_t)c * 1024 + lane * 16) = o;
        }
    }
    __syncthreads();
    SST(1);
    int wn[4]; u32 rk[4];
#pragma unroll
    for (int k = 0; k < 4; ++k) {
        wn[k] = (u32)d[k] / (u32)WN;
        if (tid + 1024 * k < CH) rk[k] = atomicAdd(&hist[wn[k]], 1u);
    }
    __syncthreads();
    SST(2);
    const u32 h0 = hist[2 * tid], h1 = hist[2 * tid + 1];
    const u32 x = wave_scan(h0 + h1);
    if (lane == 63) wsum[wv] = x;
    __syncthreads();
    const u32 wsv = wsum[lane & 15];
    const u32 wsc = wave_scan(lane < 16 ? wsv : 0u) - wsv;
    const u32 pre = __builtin_amdgcn_readlane(wsc, __builtin_amdgcn_readfirstlane(wv));
    const u32 st0 = pre + x - h0 - h1, st1 = st0 + h0;
    hist[2 * tid] = st0; hist[2 * tid + 1] = st1;
    if (tid < NWIN / 2) blk[(tid >> 2) * 32 + (tid & 3)] = st0 | (st1 << 16);
    if ((tid & 3) == 0 && tid > 0 && tid <= NWIN / 2) blk[((tid >> 2) - 1) * 32 + 4] = st0;
    __syncthreads();
    SST(3);
#pragma unroll
    for (int k = 0; k < 4; ++k) {
        if (tid + 1024 * k < CH) {
            const u32 p = hist[wn[k]] + rk[k];
            const u32 j = p - hist[wn[k] & ~7];
            const u32 r = (u32)s[k] * (u32)(WN * CH) + (u32)(d[k] - wn[k] * WN) * (u32)CH + (u32)(tid + 1024 * k);
            if (j < (u32)INL) blk[(wn[k] >> 3) * 32 + 5 + j] = r; else ((u32*)(ws + WS_REC))[e0 + p] = r;
        }
    }
    __syncthreads();
    SST(4);
#pragma unroll
    for (int k = 0; k < 2; ++k) {
        const int i = tid + 1024 * k;
        if (i < NBK * 8) *(u32x4*)(ws + WS_BLK + ((size_t)(i >> 3) * NCH + c) * 128 + (i & 7) * 16) = ((const u32x4*)blk)[i];
    }
    SST(5); SST_FLUSH();
}

constexpr int L_H = 0, L_M = 8192, L_S = 10240, L_IDX = 12288, L_SRC = 14336, L_PW = 15360, L_WAVE = 15872;
constexpr int B_LUT = 8 * L_WAVE, LUTP = 80, B_PRG = B_LUT + 2080, B_STG = B_LUT + 2176, B_BYTES = B_STG + NCH * 128;

static DI void dma16(u32 voff, __amdgpu_buffer_rsrc_t rs, u32 ldsaddr) {
    u32 keep;
    if (ABL & 16) { asm volatile("" :: "v"(voff)); return; }
    asm volatile("s_mov_b32 m0, %2\n\ts_nop 0\n\tbuffer_load_dwordx4 %0, %1, 0 offen lds"
                 :: "v"(voff), "s"(rs), "s"(ldsaddr) : "memory", "m0");
}
static DI void dma16p(u32 voff, __amdgpu_buffer_rsrc_t rs, u32 ldsaddr) {
    u32 keep;
    asm volatile("s_nop 4\n\ts_mov_b32 %0, m0\n\ts_mov_b32 m0, %3\n\ts_nop 0\n\tbuffer_load_dwordx4 %1, %2, 0 offen lds\n\ts_mov_b32 m0, %0"
                 : "=&s"(keep) : "v"(voff), "s"(rs), "s"(ldsaddr) : "memory");
}
static DI u32x4 bload16(u32 voff, __amdgpu_buffer_rsrc_t rs) {
    u32x4 r; if (ABL & 32) { asm volatile("" : "=v"(r) : "v"(voff)); return r; }
    asm volatile("buffer_load_dwordx4 %0, %1, %2, 0 offen" : "=v"(r) : "v"(voff), "s"(rs) : "memory"); return r;
}
typedef u32 u32x3 __attribute__((ext_vector_type(3)));
static DI u32x3 bload12(u32 voff, __amdgpu_buffer_rsrc_t rs) {
    u32x3 r; if (ABL & 32) { asm volatile("" : "=v"(r) : "v"(voff)); return r; }
    asm volatile("buffer_load_dwordx3 %0, %1, %2, 0 offen" : "=v"(r) : "v"(voff), "s"(rs) : "memory"); return r;
}
static DI s16x4 trread(LAS const char* p) {
    typedef short v4i16_t __attribute__((ext_vector_type(4)));
    return __builtin_bit_cast(s16x4, __builtin_amdgcn_ds_read_tr16_b64_v4i16((LAS v4i16_t*)p));
}
#define CBAR() asm volatile("" ::: "memory")
#define STAMP(i)
#define STAMP_FLUSH()
#define STAMP_DECL
#define SBAR() __builtin_amdgcn_sched_barrier(0)
static DI int lane_id() { int l; asm volatile("v_mbcnt_lo_u32_b32 %0, -1, 0\n\tv_mbcnt_hi_u32_b32 %0, -1, %0" : "=v"(l)); return l; }
static DI float silu2(float x) { return (ABL & 4) ? x : x * __builtin_amdgcn_rcpf(1.f + __builtin_amdgcn_exp2f(x)); }
static DI float dot2(u32 a, u32 b, float c) { return __builtin_amdgcn_fdot2_f32_bf16(__builtin_bit_cast(bf16x2, a), __builtin_bit_cast(bf16x2, b), c, false); }
static DI bf16x8 cat8(s16x4 a, s16x4 b) { return __builtin_shufflevector(a, b, 0, 1, 2, 3, 4, 5, 6, 7); }
static DI float smul(float a, float b) { float r = a * b; asm volatile("" : "+v"(r)); return r; }
static DI bf16x8 asb(u32x4 v) { return __builtin_bit_cast(bf16x8, v); }

__global__ __launch_bounds__(512, 2) void edge_kernel(const u16* __restrict__ hg, const u16* __restrict__ posg, const u16* __restrict__ eag,
                                                      char* __restrict__ ws, u16* __restrict__ out) {
    extern __shared__ __attribute__((aligned(16))) char smem[];
    const int lane = threadIdx.x & 63, e = lane & 31, hh = lane >> 5;
    const int wv = __builtin_amdgcn_readfirstlane(threadIdx.x >> 6);
    const int w = blockIdx.x * 8 + wv, nbase = w * WN;
    LAS char* B = (LAS char*)smem;
    LAS char* L = B + wv * L_WAVE;
    const u32 lbase = (u32)(uintptr_t)L;
    LAS u32* lidx = (LAS u32*)(L + L_IDX);
    LAS u16* lsrc = (LAS u16*)(L + L_SRC);

    const u32* rec = (const u32*)(ws + WS_REC);
    const u32x4* wpk = (const u32x4*)(ws + WS_WPK);
    const f32x4* pos4 = (const f32x4*)(ws + WS_POS);
    STAMP_DECL
    STAMP(0);

    {
        const auto rs_b = __builtin_amdgcn_make_buffer_rsrc((void*)(ws + WS_BLK + (size_t)blockIdx.x * NCH * 128), 0, NCH * 128, 0x00020000);
        const u32 vo = (u32)wv * 4096 + (lane >> 3) * 128 + (((lane & 7) ^ ((lane >> 3) & 7)) << 4);
        const u32 sb = (u32)(uintptr_t)(B + B_STG) + wv * 4096;
#pragma unroll
        for (int q = 0; q < 4; ++q) dma16p(vo + q * 1024, rs_b, sb + q * 1024);
    }
    {
        const auto rs_w = __builtin_amdgcn_make_buffer_rsrc((void*)(ws + WS_WPK), 0, NFRAG * 1024, 0x00020000);
#pragma unroll
        for (int q = 0; q < 3; ++q) {
            const int i = wv * 3 + q, f = i < 18 ? i : i + 16;
            dma16p((u32)f * 1024 + lane * 16, rs_w, (u32)(uintptr_t)B + (i >> 2) * L_WAVE + 4096 + (i & 3) * 1024);
        }
    }
    f32x16 y0 = {0}, y1 = {0}; float yp[3] = {0.f, 0.f, 0.f};
    bf16x8 wf[10], an[4], wxf[4], hw[4];
    {
        const int nn = nbase + (e < WN ? e : WN - 1);
#pragma unroll
        for (int s = 0; s < 4; ++s) hw[s] = *(const bf16x8*)(hg + (size_t)nn * 64 + 16 * s + 8 * hh);
        const f32x4 pw = pos4[nn];
#pragma unroll
        for (int i = 0; i < 4; ++i) *(LAS u32x4*)(L + L_M + i * 1024 + lane * 16) = (u32x4){0, 0, 0, 0};
        for (int i = threadIdx.x; i < 26 * 32; i += 512) {
            const int row = i >> 5, n = i & 31;
            *(LAS u16*)(B + B_LUT + row * LUTP + (2 * (n >> 4) + ((n >> 2) & 1)) * 16 + (((n >> 3) & 1) * 4 + (n & 3)) * 2) = (n == row && row < WN) ? (u16)0x3F80 : (u16)0;
        }
        if (lane == 0) *(LAS int*)(B + B_PRG + wv * 4) = 0;
        if (threadIdx.x < 2) *(LAS int*)(B + B_PRG + 32 + threadIdx.x * 4) = 0;
        if (lane < 32) *(LAS f32x4*)(L + L_PW + lane * 16) = lane < WN ? pw : (f32x4){0.f, 0.f, 0.f, 0.f};
        asm volatile("" : "+v"(hw[0]), "+v"(hw[1]), "+v"(hw[2]), "+v"(hw[3]));
        asm volatile("s_waitcnt vmcnt(0) lgkmcnt(0)\n\ts_barrier" ::: "memory");
        STAMP(1);
    }
    const u32 fsw = (e >> 1) & 7;
    LAS const char* hrd[4];
#pragma unroll
    for (int s = 0; s < 4; ++s) hrd[s] = L + L_H + e * 128 + (((2 * s + hh) ^ fsw) << 4);
    const u32 swz0 = ((lane & 7) ^ (lane >> 4)) << 4, swz1 = swz0 ^ 64;
    LAS char* mwr[4];
#pragma unroll
    for (int g = 0; g < 4; ++g) mwr[g] = L + L_M + e * 64 + (((2 * g + hh) ^ fsw) << 3);
    const int i16 = lane & 15, tq = i16 >> 2, tp = i16 & 3, tb = (lane >> 4) & 1;
    LAS const char* mrd[4];
#pragma unroll
    for (int i = 0; i < 4; ++i) {
        const int row = 16 * (i >> 1) + 8 * hh + 4 * (i & 1) + tq;
        mrd[i] = L + L_M + row * 64 + (((4 * tb + tp) ^ ((row >> 1) & 7)) << 3);
    }
    LAS const char* srd[2];
#pragma unroll
    for (int s = 0; s < 2; ++s) srd[s] = L + L_S + e * 64 + (((2 * s + hh) ^ ((e >> 2) & 3)) << 4);

    int T = 0; bool whl = false; u32x2 hv[8];
    for (int b0 = 0; b0 == 0 || b0 < T; b0 += CAP) {
        int nb, nt;
        {
            const int ln = lane_id();
            LAS const char* sg = B + B_STG + ln * 128;
            const u32 sx = (ln & 7) << 4;
            u32 lo_[4], cn_[4], s0_[4];
#pragma unroll
            for (int q = 0; q < 4; ++q) {
                const u32 a = *(LAS const u16*)(sg + q * 8192 + ((2 * wv) ^ sx)), b = *(LAS const u16*)(sg + q * 8192 + ((2 * wv + 2) ^ sx));
                s0_[q] = *(LAS const u16*)(sg + q * 8192 + sx);
                lo_[q] = a; cn_[q] = b - a;
            }
            const u32 ltot = cn_[0] + cn_[1] + cn_[2] + cn_[3];
            const u32 inc = wave_scan(ltot);
            T = __builtin_amdgcn_readlane((int)inc, 63);
            nb = (T - b0) < CAP ? (T - b0) : CAP;
            nt = (nb + 31) >> 5;
            const u32 mx = max(max(cn_[0], cn_[1]), max(cn_[2], cn_[3]));
            u32 pp[4];
            pp[0] = inc - ltot - (u32)b0; pp[1] = pp[0] + cn_[0]; pp[2] = pp[1] + cn_[1]; pp[3] = pp[2] + cn_[2];
            u32 lc[4];
#pragma unroll
            for (int q = 0; q < 4; ++q) lc[q] = ((u32)(ln + 64 * q) << 12) + lo_[q] - s0_[q];
#pragma clang loop unroll(disable) vectorize(disable)
            for (u32 j = 0; j < mx; ++j) {
#pragma unroll
                for (int q = 0; q < 4; ++q) if (j < cn_[q] && pp[q] + j < (u32)CAP) lidx[pp[q] + j] = lc[q] + j;
            }
            CBAR();
            {
                u32 lo2[8], r[8]; bool ovf = false;
#pragma unroll
                for (int k = 0; k < 8; ++k) { const u32 v = lidx[64 * k + ln]; lo2[k] = 64 * k + ln < nb ? v : 0u; }
#pragma unroll
                for (int k = 0; k < 8; ++k) {
                    const u32 cc = lo2[k] >> 12, jj = lo2[k] & 4095;
                    r[k] = *(LAS const u32*)(B + B_STG + cc * 128 + ((20 + 4 * (jj < (u32)INL ? jj : 0u)) ^ ((cc & 7) << 4)));
                    ovf |= jj >= (u32)INL;
                }
                if (__builtin_amdgcn_ballot_w64(ovf)) {
#pragma unroll
                    for (int k = 0; k < 8; ++k) {
                        const u32 cc = lo2[k] >> 12, jj = lo2[k] & 4095;
                        if (jj >= (u32)INL) r[k] = rec[cc * CH + *(LAS const u16*)(B + B_STG + cc * 128 + ((cc & 7) << 4)) + jj];
                    }
                }
#pragma unroll
                for (int k = 0; k < 8; ++k) {
                    const int i = 64 * k + ln;
                    const u32 sv = r[k] / (u32)(WN * CH), rm = r[k] - sv * (u32)(WN * CH), dv = rm / (u32)CH;
                    const bool ok = i < nb;
                    lidx[i] = ok ? ((lo2[k] >> 12) * CH + rm - dv * (u32)CH) | (dv << 20) : ((u32)WN << 20);
                    lsrc[i] = ok ? (u16)sv : (u16)0;
                }
            }
        }
        CBAR();
        STAMP(2);

        u32x4 eaA, eaB; u32x3 psA, psB;
        const int ln2 = lane_id();
        LAS const u16* lsr8 = lsrc + (ln2 >> 3);
        LAS const u16* lsre = lsrc + (ln2 & 31);
        LAS const u32* lide = lidx + (ln2 & 31);
#define ISSUE_D(TT, BUF) do { \
            const int tb_ = (TT) * 32; \
            const u32 s0_ = lsr8[tb_], s1_ = lsr8[tb_ + 8], s2_ = lsr8[tb_ + 16], s3_ = lsr8[tb_ + 24]; \
            const auto rs_h = __builtin_amdgcn_make_buffer_rsrc((void*)hg, 0, ((TT) < nt && !(ABL & 1)) ? N_ * 128 : 0, 0x00020000); \
            dma16(s0_ * 128 + swz0, rs_h, lbase + L_H + (BUF) * 4096); \
            dma16(s1_ * 128 + swz1, rs_h, lbase + L_H + (BUF) * 4096 + 1024); \
            dma16(s2_ * 128 + swz0, rs_h, lbase + L_H + (BUF) * 4096 + 2048); \
            dma16(s3_ * 128 + swz1, rs_h, lbase + L_H + (BUF) * 4096 + 3072); \
        } while (0)
#define ISSUE_E(TT, EA, PS) do { \
            const u32 li_ = lide[(TT) * 32], so_ = lsre[(TT) * 32]; \
            const auto rs_e = __builtin_amdgcn_make_buffer_rsrc((void*)eag, 0, ((TT) < nt && !(ABL & 2)) ? E_ * 32 : 0, 0x00020000); \
            const auto rs_p = __builtin_amdgcn_make_buffer_rsrc((void*)(ws + WS_POS), 0, ((TT) < nt && !(ABL & 2)) ? N_ * 16 : 0, 0x00020000); \
            EA = bload16((li_ & 0xFFFFFu) * 32 + hh * 16, rs_e); \
            PS = bload12(so_ * 16, rs_p); \
        } while (0)
#define MFMA16(a, b, c) __builtin_amdgcn_mfma_f32_16x16x32_bf16((a), (b), (c), 0, 0, 0)
#define PIN4(a, b, c, d) asm volatile("" : "+v"(a), "+v"(b), "+v"(c), "+v"(d))
#define SGROUP(C, PK, G, SA, SB, SC) do { \
            float e0_ = C[4 * (G)], e1_ = C[4 * (G) + 1], e2_ = C[4 * (G) + 2], e3_ = C[4 * (G) + 3]; \
            if (!(ABL & 4)) { e0_ = __builtin_amdgcn_exp2f(e0_); e1_ = __builtin_amdgcn_exp2f(e1_); e2_ = __builtin_amdgcn_exp2f(e2_); e3_ = __builtin_amdgcn_exp2f(e3_); } \
            PIN4(e0_, e1_, e2_, e3_); \
            SA \
            PIN4(e0_, e1_, e2_, e3_); \
            if (!(ABL & 4)) { e0_ = 1.f + e0_; asm volatile("" : "+v"(e0_)); e1_ = 1.f + e1_; asm volatile("" : "+v"(e1_)); e2_ = 1.f + e2_; asm volatile("" : "+v"(e2_)); e3_ = 1.f + e3_; \
            e0_ = __builtin_amdgcn_rcpf(e0_); e1_ = __builtin_amdgcn_rcpf(e1_); } \
            PIN4(e0_, e1_, e2_, e3_); \
            SB \
            PIN4(e0_, e1_, e2_, e3_); \
            if (!(ABL & 4)) { e2_ = __builtin_amdgcn_rcpf(e2_); e3_ = __builtin_amdgcn_rcpf(e3_); \
            e0_ = smul(e0_, C[4 * (G)]); e1_ = smul(e1_, C[4 * (G) + 1]); \
            e2_ = smul(e2_, C[4 * (G) + 2]); e3_ = smul(e3_, C[4 * (G) + 3]); } \
            PK[2 * (G)] = cvtpk(e0_, e1_); PK[2 * (G) + 1] = cvtpk(e2_, e3_); \
            asm volatile("" : "+v"(PK[2 * (G)]), "+v"(PK[2 * (G) + 1])); \
            SC \
        } while (0)
#define M32(D, A_, B_) { asm volatile("" : "+v"(D)); D = MFMA(A_, B_, D); asm volatile("" : "+v"(D)); }
#define M16(D, A_, B_) { asm volatile("" : "+v"(D)); D = MFMA16(A_, B_, D); asm volatile("" : "+v"(D)); }
#define M32Z(D, A_, B_, P_) { asm volatile("" : "+v"(P_)); D = MFMA(A_, B_, zero16); asm volatile("" : "+v"(D)); }
#define M16Z(D, A_, B_, P_) { asm volatile("" : "+v"(P_)); D = MFMA16(A_, B_, zero4); asm volatile("" : "+v"(D)); }
#define FETCH(TT, BUF, EA, PS) \
            asm volatile("s_waitcnt vmcnt(6)" : "+v"(EA), "+v"(PS) :: "memory"); \
            dl = dln < WN ? dln : WN; \
            _Pragma("unroll") for (int s = 0; s < 4; ++s) xb[s] = *(LAS const bf16x8*)(hrd[s] + (BUF) * 4096); \
            { LAS const char* lp_ = B + B_LUT + dl * LUTP + hh * 16; \
              oh0 = *(LAS const u32x4*)lp_; oh1 = *(LAS const u32x4*)(lp_ + 32); \
              const f32x4 pd_ = *(LAS const f32x4*)(L + L_PW + dl * 16); ndx = pd_[0]; ndy = pd_[1]; ndz = pd_[2]; } \
            dln = lide[((TT) + 1) * 32] >> 20;
#define GEOM(PS) \
            ndx -= __uint_as_float(PS[0]); asm volatile("" : "+v"(ndx)); ndy -= __uint_as_float(PS[1]); asm volatile("" : "+v"(ndy)); ndz -= __uint_as_float(PS[2]); \
            { const float d2 = __builtin_fmaf(ndx, ndx, __builtin_fmaf(ndy, ndy, smul(ndz, ndz))); const u32 dp_ = cvtpk(d2, d2); oh1[3] = hh ? dp_ : oh1[3]; }

        const f32x16 zero16 = {0};
        f32x16 c0, c1; bf16x8 xb[4]; u32x4 oh0, oh1;
        bf16x8 sf0 = {0}, sf1 = {0}; s16x4 u0 = {0}, u1 = {0}, u2 = {0}, u3 = {0};
        const f32x4 zero4 = {0.f, 0.f, 0.f, 0.f}; f32x4 cz = zero4; u32 pk1[8] = {0u, 0u, 0u, 0u, 0u, 0u, 0u, 0u};
        float dx = 0.f, dy = 0.f, dz = 0.f, ndx, ndy, ndz; u32 dl, dln;

        ISSUE_D(0, 0); ISSUE_E(0, eaA, psA); ISSUE_E(1, eaB, psB);
        if (b0 == 0) {
#define WFR(i) (*(LAS const u32x4*)(B + ((i) >> 2) * L_WAVE + 4096 + ((i) & 3) * 1024 + lane * 16))
        f32x16 a0 = {0}, a1 = {0};
#pragma unroll
        for (int s = 0; s < 4; ++s) {
            a0 = MFMA(hw[s], asb(WFR(10 + s)), a0);
            a1 = MFMA(hw[s], asb(WFR(14 + s)), a1);
        }
        const u32x4 wd = WFR(18);
#pragma unroll
        for (int i = 0; i < 10; ++i) wf[i] = asb(WFR(i));
#pragma unroll
        for (int i = 0; i < 4; ++i) wxf[i] = asb(WFR(19 + i));
        u32 p0[8], p1[8];
#pragma unroll
        for (int i = 0; i < 8; ++i) { p0[i] = cvtpk(a0[2 * i] * NLOG2E, a0[2 * i + 1] * NLOG2E); p1[i] = cvtpk(a1[2 * i] * NLOG2E, a1[2 * i + 1] * NLOG2E); }
        if (hh) { p0[7] = wd[0]; p1[7] = wd[1]; }
        an[0] = asb((u32x4){p0[0], p0[1], p0[2], p0[3]}); an[1] = asb((u32x4){p0[4], p0[5], p0[6], p0[7]});
        an[2] = asb((u32x4){p1[0], p1[1], p1[2], p1[3]}); an[3] = asb((u32x4){p1[4], p1[5], p1[6], p1[7]});
#pragma unroll
        for (int i = 0; i < 10; ++i) asm volatile("" : "+v"(wf[i]));
#pragma unroll
        for (int i = 0; i < 4; ++i) asm volatile("" : "+v"(wxf[i]), "+v"(an[i]));
        if (T > CAP) *(LAS volatile int*)(B + B_PRG + 32) = 1;
        asm volatile("s_waitcnt lgkmcnt(0)\n\ts_barrier" ::: "memory");
        whl = __builtin_amdgcn_readfirstlane(*(LAS volatile int*)(B + B_PRG + 32)) == 0;
        {
            const auto rs_w = __builtin_amdgcn_make_buffer_rsrc((void*)(ws + WS_WPK), 0, whl ? NFRAG * 1024 : 0, 0x00020000);
            const u32 dsta = whl ? (u32)(uintptr_t)(B + B_STG) + wv * 2048 : lbase + L_H + 4096;
            dma16p((u32)(18 + 2 * wv) * 1024 + lane * 16, rs_w, dsta);
            dma16p((u32)(19 + 2 * wv) * 1024 + lane * 16, rs_w, dsta + 1024);
        }
        }
        ISSUE_D(1, 1);
        if (b0 == 0) {
            asm volatile("s_waitcnt vmcnt(4)" ::: "memory");
            if (lane == 0) __hip_atomic_fetch_add((LAS int*)(B + B_PRG + 36), 1, __ATOMIC_RELAXED, __HIP_MEMORY_SCOPE_WORKGROUP);
        }
        dln = lide[0] >> 20;
        FETCH(0, 0, eaA, psA)
        STAMP(3);
        GEOM(psA)
        asm volatile("" : "+v"(xb[0]), "+v"(xb[1]), "+v"(xb[2]), "+v"(xb[3]) :: "memory");
        ISSUE_D(2, 0);
        c0 = MFMA(wf[4], asb(eaA), zero16);
#pragma unroll
        for (int s = 0; s < 4; ++s) c0 = MFMA(wf[s], xb[s], c0);
        c0 = MFMA(an[0], asb(oh0), c0);
        c0 = MFMA(an[1], asb(oh1), c0);

#define PKV(P, O) asb((u32x4){P[O], P[(O) + 1], P[(O) + 2], P[(O) + 3]})
#define POSV() { const float csv = cz[0] * NLN2; \
              *(LAS u32x2*)mwr[0] = (u32x2){cvtpk(smul(csv, dx), smul(csv, dy)), cvtpk(smul(csv, dz), 0.f)}; \
              CBAR(); \
              q0 = trread(mrd[0]); q1 = trread(mrd[1]); q2 = trread(mrd[2]); q3 = trread(mrd[3]); \
              CBAR(); }
#define BODY(TT, BUF, EA, PS, EAN, PSN) do { \
            u32 pk0[8]; s16x4 q0, q1, q2, q3; f32x16 yt; \
            LAS u16* sp_ = (LAS u16*)(L + L_S + dl * 64 + ((((u32)e >> 3) ^ ((dl >> 2) & 3)) << 4) + (e & 7) * 2); \
            *sp_ = (u16)0x3F80; \
            CBAR(); \
              \
            SGROUP(c0, pk0, 0, M16(cz, wxf[3], PKV(pk1, 4)), M32Z(c1, wf[9], asb(EA), EA), M32(c1, wf[5], xb[0])); \
            POSV(); \
            dx = ndx; dy = ndy; dz = ndz; \
            ISSUE_E((TT) + 2, EA, PS); \
            SGROUP(c0, pk0, 1, M32(c1, wf[6], xb[1]), M32(y1, cat8(u0, u1), sf0), M32(c1, wf[7], xb[2])); \
            SGROUP(c0, pk0, 2, M32(y1, cat8(u2, u3), sf1), M32(c1, wf[8], xb[3]), M32(c1, an[2], asb(oh0))); \
            SGROUP(c0, pk0, 3, M32(c1, an[3], asb(oh1)), M32Z(yt, cat8(q0, q1), sf0, sf0), M32(yt, cat8(q2, q3), sf1)); \
            CBAR(); \
            sf0 = *(LAS const bf16x8*)srd[0]; sf1 = *(LAS const bf16x8*)srd[1]; \
            CBAR(); \
            *sp_ = (u16)0; \
            _Pragma("unroll") for (int g = 0; g < 4; ++g) *(LAS u32x2*)mwr[g] = (u32x2){pk0[2 * g], pk0[2 * g + 1]}; \
            CBAR(); \
            const s16x4 t0 = trread(mrd[0]), t1 = trread(mrd[1]), t2 = trread(mrd[2]), t3 = trread(mrd[3]); \
            CBAR(); \
              \
            SGROUP(c1, pk1, 0, M16Z(cz, wxf[0], PKV(pk0, 0), pk0[0]), M16(cz, wxf[1], PKV(pk0, 4)), \
                   { yp[0] += yt[0]; yp[1] += yt[1]; yp[2] += yt[2]; asm volatile("" : "+v"(yp[0]), "+v"(yp[1]), "+v"(yp[2])); } ); \
            FETCH((TT) + 1, (BUF) ^ 1, EAN, PSN) \
            M32Z(c0, wf[4], asb(EAN), EAN) \
            SGROUP(c1, pk1, 1, M32(y0, cat8(t0, t1), sf0), M32(y0, cat8(t2, t3), sf1), M32(c0, wf[0], xb[0])); \
            GEOM(PSN) \
            asm volatile("" : "+v"(xb[0]), "+v"(xb[1]), "+v"(xb[2]), "+v"(xb[3]) :: "memory"); \
            ISSUE_D((TT) + 3, (BUF) ^ 1); \
            SGROUP(c1, pk1, 2, M32(c0, wf[1], xb[1]), M32(c0, wf[2], xb[2]), M32(c0, wf[3], xb[3])); \
            SGROUP(c1, pk1, 3, M32(c0, an[0], asb(oh0)), M32(c0, an[1], asb(oh1)), M16(cz, wxf[2], PKV(pk1, 0))); \
            CBAR(); \
            _Pragma("unroll") for (int g = 0; g < 4; ++g) *(LAS u32x2*)mwr[g] = (u32x2){pk1[2 * g], pk1[2 * g + 1]}; \
            CBAR(); \
            u0 = trread(mrd[0]); u1 = trread(mrd[1]); u2 = trread(mrd[2]); u3 = trread(mrd[3]); \
            CBAR(); \
        } while (0)

        LAS int* prg = (LAS int*)(B + B_PRG);
        int other = 0;
        for (int t = 0; t < nt; t += 2) {
            if (PRIO) {
                if (T - b0 - 32 * t + PBIAS > __builtin_amdgcn_readfirstlane(other)) __builtin_amdgcn_s_setprio(1); else __builtin_amdgcn_s_setprio(0);
                prg[wv] = T - b0 - 32 * t;
                other = prg[wv ^ 4];
            }
            BODY(t, 0, eaA, psA, eaB, psB);
            if (t + 1 < nt) BODY(t + 1, 1, eaB, psB, eaA, psA);
        }
        if (PRIO) { prg[wv] = T - b0 - 32 * nt; __builtin_amdgcn_s_setprio(0); }
        {
            CBAR();
            const int l3 = lane_id(), e3 = l3 & 31, h3 = l3 >> 5;
            const int nn = nbase + (e3 < WN ? e3 : WN - 1);
#pragma unroll
            for (int s = 0; s < 4; ++s) hw[s] = *(const bf16x8*)(hg + (size_t)nn * 64 + 16 * s + 8 * h3);
#pragma unroll
            for (int g = 0; g < 8; ++g) hv[g] = *(const u32x2*)(hg + (size_t)nn * 64 + 32 * (g >> 2) + 8 * (g & 3) + 4 * h3);
            CBAR();
        }
        {
            s16x4 q0, q1, q2, q3;
            cz = MFMA16(wxf[3], PKV(pk1, 4), cz);
            y1 = MFMA(cat8(u0, u1), sf0, y1);
            y1 = MFMA(cat8(u2, u3), sf1, y1);
            CBAR();
            POSV();
            f32x16 yt = MFMA(cat8(q0, q1), sf0, zero16);
            yt = MFMA(cat8(q2, q3), sf1, yt);
            yp[0] += yt[0]; yp[1] += yt[1]; yp[2] += yt[2];
        }
        asm volatile("s_waitcnt vmcnt(0)" : "+v"(eaA), "+v"(psA), "+v"(eaB), "+v"(psB) :: "memory");
        STAMP(4);
    }

    {
        CBAR();
        const int lane = lane_id();
        const int e = lane & 31, hh = lane >> 5;
        const int nn = nbase + (e < WN ? e : WN - 1);
        u32 p0[8], p1[8];
#pragma unroll
        for (int i = 0; i < 8; ++i) { p0[i] = cvtpk(y0[2 * i] * NLN2, y0[2 * i + 1] * NLN2); p1[i] = cvtpk(y1[2 * i] * NLN2, y1[2 * i + 1] * NLN2); }
        bf16x8 mb[4];
        mb[0] = asb((u32x4){p0[0], p0[1], p0[2], p0[3]});
        mb[1] = asb((u32x4){p0[4], p0[5], p0[6], p0[7]});
        mb[2] = asb((u32x4){p1[0], p1[1], p1[2], p1[3]});
        mb[3] = asb((u32x4){p1[4], p1[5], p1[6], p1[7]});
        f32x16 o0 = {0}, o1 = {0};
        if (__builtin_amdgcn_readfirstlane(*(LAS volatile int*)(B + B_PRG + 36)) < 8) whl = false;
        const u32x4* whp = whl ? (const u32x4*)(char*)(B + B_STG) + lane : wpk + 18 * 64 + lane;
#pragma unroll
        for (int s = 0; s < 4; ++s) {
            o0 = MFMA(asb(whp[(8 + s) * 64]), mb[s], o0);
            o1 = MFMA(asb(whp[(12 + s) * 64]), mb[s], o1);
        }
#pragma unroll
        for (int s = 0; s < 4; ++s) {
            o0 = MFMA(asb(whp[s * 64]), hw[s], o0);
            o1 = MFMA(asb(whp[(4 + s) * 64]), hw[s], o1);
        }
        if (e < WN) {
#pragma unroll
            for (int g = 0; g < 8; ++g) {
                const int f = 32 * (g >> 2) + 8 * (g & 3) + 4 * hh;
                float r[4];
#pragma unroll
                for (int i = 0; i < 4; ++i) {
                    const float a = g < 4 ? o0[4 * (g & 3) + i] : o1[4 * (g & 3) + i];
                    const float sv = a * __builtin_amdgcn_rcpf(1.f + __builtin_amdgcn_exp2f(a * NLOG2E));
                    const u32 hw_ = hv[g][i >> 1];
                    const float hf = __uint_as_float((i & 1) ? (hw_ & 0xFFFF0000u) : (hw_ << 16));
                    r[i] = hf + sv;
                }
                *(u32x2*)(out + (size_t)nn * 64 + f) = (u32x2){cvtpk(r[0], r[1]), cvtpk(r[2], r[3])};
            }
        }
        {
            const float ox = yp[0], oy = yp[1], oz = yp[2];
            if (e < WN && hh == 0) {
                const size_t o = (size_t)nn * 3;
                u16* po = out + (size_t)N_ * 64 + o;
                const f32x4 pw = *(LAS const f32x4*)(L + L_PW + e * 16);
                po[0] = f2bf(pw[0] + ox); po[1] = f2bf(pw[1] + oy); po[2] = f2bf(pw[2] + oz);
            }
        }
    }
    STAMP(5);
    STAMP_FLUSH();
}

__global__ __launch_bounds__(256) void g_edge_kernel(const u16* h, const u16* pos, const u16* ea, const int* src, const int* dst,
                                                     const u16* we, const u16* wx, float* macc, float* pacc, int E) {
    __shared__ float sw[145 * 64];
    for (int i = threadIdx.x; i < 145 * 64; i += blockDim.x) sw[i] = bf2f(we[i]);
    __syncthreads();
    int lane = threadIdx.x & 63;
    int wid = (blockIdx.x * blockDim.x + threadIdx.x) >> 6;
    int nw = (gridDim.x * blockDim.x) >> 6;
    float wxl = bf2f(wx[lane]);
    for (int e = wid; e < E; e += nw) {
        int s = src[e], d = dst[e];
        float dx = bf2f(pos[(size_t)d * 3 + 0]) - bf2f(pos[(size_t)s * 3 + 0]);
        float dy = bf2f(pos[(size_t)d * 3 + 1]) - bf2f(pos[(size_t)s * 3 + 1]);
        float dz = bf2f(pos[(size_t)d * 3 + 2]) - bf2f(pos[(size_t)s * 3 + 2]);
        float d2 = dx * dx + dy * dy + dz * dz;
        float hd = bf2f(h[(size_t)d * 64 + lane]), hs = bf2f(h[(size_t)s * 64 + lane]);
        float eav = lane < 16 ? bf2f(ea[(size_t)e * 16 + lane]) : 0.f;
        float acc = d2 * sw[128 * 64 + lane];
#pragma unroll 8
        for (int k = 0; k < 64; ++k) acc += __shfl(hd, k) * sw[k * 64 + lane];
#pragma unroll 8
        for (int k = 0; k < 64; ++k) acc += __shfl(hs, k) * sw[(64 + k) * 64 + lane];
#pragma unroll 8
        for (int k = 0; k < 16; ++k) acc += __shfl(eav, k) * sw[(129 + k) * 64 + lane];
        float m = acc / (1.f + __expf(-acc));
        float cs = m * wxl;
        for (int o = 32; o > 0; o >>= 1) cs += __shfl_xor(cs, o);
        atomicAdd(&macc[(size_t)d * 64 + lane], m);
        if (lane < 3) atomicAdd(&pacc[(size_t)d * 3 + lane], (lane == 0 ? dx : lane == 1 ? dy : dz) * cs);
    }
}
__global__ __launch_bounds__(256) void g_node_kernel(const u16* h, const u16* pos, const u16* wh, const float* macc, const float* pacc,
                                                     u16* out, int N) {
    __shared__ float sw[128 * 64];
    for (int i = threadIdx.x; i < 128 * 64; i += blockDim.x) sw[i] = bf2f(wh[i]);
    __syncthreads();
    int lane = threadIdx.x & 63;
    int wid = (blockIdx.x * blockDim.x + threadIdx.x) >> 6;
    int nw = (gridDim.x * blockDim.x) >> 6;
    for (int n = wid; n < N; n += nw) {
        float hv = bf2f(h[(size_t)n * 64 + lane]);
        float mv = macc[(size_t)n * 64 + lane];
        float acc = 0.f;
#pragma unroll 8
        for (int k = 0; k < 64; ++k) acc += __shfl(hv, k) * sw[k * 64 + lane];
#pragma unroll 8
        for (int k = 0; k < 64; ++k) acc += __shfl(mv, k) * sw[(64 + k) * 64 + lane];
        float s = acc / (1.f + __expf(-acc));
        out[(size_t)n * 64 + lane] = f2bf(hv + s);
        if (lane < 3) out[(size_t)N * 64 + (size_t)n * 3 + lane] = f2bf(bf2f(pos[(size_t)n * 3 + lane]) + pacc[(size_t)n * 3 + lane]);
    }
}

extern "C" void kernel_launch(void* const* d_in, const int* in_sizes, int n_in,
                              void* d_out, int out_size, void* d_ws, size_t ws_size,
                              hipStream_t stream) {
    const u16* h = (const u16*)d_in[0];
    const u16* pos = (const u16*)d_in[1];
    const u16* ea = (const u16*)d_in[2];
    const int* src = (const int*)d_in[3];
    const int* dst = (const int*)d_in[4];
    const u16* we = (const u16*)d_in[5];
    const u16* wx = (const u16*)d_in[6];
    const u16* wh = (const u16*)d_in[7];
    const int N = in_sizes[0] / 64, E = in_sizes[3];
    if (N == N_ && E == E_ && ws_size >= WS_END) {
        (void)hipFuncSetAttribute((const void*)sort_kernel, hipFuncAttributeMaxDynamicSharedMemorySize, S_BYTES);
        (void)hipFuncSetAttribute((const void*)edge_kernel, hipFuncAttributeMaxDynamicSharedMemorySize, B_BYTES);
        sort_kernel<<<NCH, 1024, S_BYTES, stream>>>(src, dst, pos, we, wx, wh, (char*)d_ws);
        edge_kernel<<<NBK, 512, B_BYTES, stream>>>(h, pos, ea, (char*)d_ws, (u16*)d_out);
    } else {
        float* macc = (float*)d_ws;
        float* pacc = macc + (size_t)N * 64;
        (void)hipMemsetAsync(d_ws, 0, (size_t)N * 67 * 4, stream);
        g_edge_kernel<<<2048, 256, 0, stream>>>(h, pos, ea, src, dst, we, wx, macc, pacc, E);
        g_node_kernel<<<1024, 256, 0, stream>>>(h, pos, wh, macc, pacc, (u16*)d_out, N);
    }
}
```

```cpp
#include <hip/hip_runtime.h>
#include <hip/hip_bf16.h>
#include <stdint.h>

#define PRIO 1
#define PBIAS 96
#define EANT 0
#define SRCORD 1
#define PREF 0
#define BAL 0
#define ABL 0
typedef unsigned short u16;
typedef unsigned int u32;
typedef u32 u32x4 __attribute__((ext_vector_type(4)));
typedef u32 u32x2 __attribute__((ext_vector_type(2)));
typedef float f32x4 __attribute__((ext_vector_type(4)));
typedef float f32x16 __attribute__((ext_vector_type(16)));
typedef short bf16x8 __attribute__((ext_vector_type(8)));
typedef short s16x4 __attribute__((ext_vector_type(4)));
#define LAS __attribute__((address_space(3)))
#define DI __device__ __forceinline__
static __device__ __forceinline__ f32x16 mfma_w(bf16x8 a, bf16x8 b, f32x16 c) {
    if (ABL & 64) { asm volatile("" : "+v"(c) : "v"(a), "v"(b)); return c; }
    return __builtin_amdgcn_mfma_f32_32x32x16_bf16(a, b, c, 0, 0, 0); }
#define MFMA(a, b, c) mfma_w((a), (b), (c))

static DI float bf2f(u16 v) { return __uint_as_float(((u32)v) << 16); }
static DI u16 f2bf(float f) { __hip_bfloat16 b = __float2bfloat16(f); return *reinterpret_cast<u16*>(&b); }
typedef float f32x2 __attribute__((ext_vector_type(2)));
typedef __bf16 bf16x2 __attribute__((ext_vector_type(2)));
static DI u32 cvtpk(float lo, float hi) { return __builtin_bit_cast(u32, __builtin_convertvector((f32x2){lo, hi}, bf16x2)); }

static DI u32 wave_scan(u32 x) {
    x += __builtin_amdgcn_update_dpp(0u, x, 0x111, 0xf, 0xf, false);
    x += __builtin_amdgcn_update_dpp(0u, x, 0x112, 0xf, 0xf, false);
    x += __builtin_amdgcn_update_dpp(0u, x, 0x114, 0xf, 0xf, false);
    x += __builtin_amdgcn_update_dpp(0u, x, 0x118, 0xf, 0xf, false);
    x += __builtin_amdgcn_update_dpp(0u, x, 0x142, 0xa, 0xf, false);
    x += __builtin_amdgcn_update_dpp(0u, x, 0x143, 0xc, 0xf, false);
    return x;
}

constexpr int N_ = 50000, E_ = 800000;
constexpr int WN = 25;
constexpr int NWIN = 2000;
constexpr int NBK = NWIN / 8;
constexpr int NCH = 256;
constexpr int CH = 3125;
constexpr int INL = 27;
constexpr int CAP = 512;
constexpr int NFRAG = 39;
#define NPRE 13
constexpr float NLOG2E = -1.4426950408889634f;
constexpr float NLN2 = -0.6931471805599453f;

constexpr size_t WS_BLK = 0;
constexpr size_t WS_REC = WS_BLK + (size_t)NBK * NCH * 128;
constexpr size_t WS_POS = WS_REC + (size_t)E_ * 4;
constexpr size_t WS_WPK = WS_POS + (size_t)N_ * 16;
constexpr size_t WS_END = WS_WPK + (size_t)NFRAG * 1024;

constexpr int S_HIST = 0, S_WSUM = 8192, S_BLK = 8192 + 128, S_BYTES = S_BLK + NBK * 128;

#define SST(i)
#define SST_DECL
#define SST_FLUSH()
__global__ __launch_bounds__(1024) void sort_kernel(const int* __restrict__ src, const int* __restrict__ dst,
                                                    const u16* __restrict__ pos, const u16* __restrict__ we, const u16* __restrict__ wx,
                                                    const u16* __restrict__ wh, char* __restrict__ ws) {
    extern __shared__ __attribute__((aligned(16))) char smem[];
    u32* hist = (u32*)(smem + S_HIST);
    u32* wsum = (u32*)(smem + S_WSUM);
    u32* blk = (u32*)(smem + S_BLK);
    const int tid = threadIdx.x, lane = tid & 63, wv = tid >> 6, c = blockIdx.x;
    const int e0 = c * CH;
    SST_DECL SST(0);
    int d[4], s[4];
#pragma unroll
    for (int k = 0; k < 4; ++k) { int i = tid + 1024 * k; i = i < CH ? i : CH - 1; d[k] = dst[e0 + i]; s[k] = src[e0 + i]; }
    hist[tid] = 0; hist[tid + 1024] = 0;

    {
        int n = c * 196 + tid;
        if (tid < 196 && n < N_) {
            f32x4 p = {bf2f(pos[n * 3]), bf2f(pos[n * 3 + 1]), bf2f(pos[n * 3 + 2]), 0.f};
            *(f32x4*)(ws + WS_POS + (size_t)n * 16) = p;
        }
        if (c < NFRAG && wv == 15) {
            const int r = lane & 31, h = lane >> 5;
            const u16* sp; int base, perm = 0, mode = 0;
            if (c < 10) { const int t = c / 5, st = c % 5; sp = we; base = (st < 4 ? 64 + 16 * st : 129) * 64 + 32 * t; mode = 1; }
            else if (c < 18) { sp = we; base = 16 * ((c - 10) & 3) * 64 + 32 * ((c - 10) >> 2); }
            else if (c < 26) { sp = wh; base = 16 * ((c - 18) & 3) * 64 + 32 * ((c - 18) >> 2); }
            else if (c < 34) { sp = wh; base = (64 + 16 * ((c - 26) & 3)) * 64 + 32 * ((c - 26) >> 2); perm = 1; }
            else if (c == 34) { sp = we; base = 128 * 64; mode = 2; }
            else { sp = wx; base = 16 * (c - 35); perm = 1; mode = 3; }
            u16 v[8];
#pragma unroll
            for (int j = 0; j < 8; ++j) {
                const int k = perm ? 8 * (j >> 2) + 4 * h + (j & 3) : 8 * h + j;
                const int idx = mode == 2 ? 32 * ((j >> 1) & 1) + r : (mode == 3 ? k : k * 64 + r);
                v[j] = sp[base + idx];
            }
            const bool on = ((lane & 15) & 7) == 4 * ((lane >> 4) & 1);
#pragma unroll
            for (int j = 0; j < 8; ++j) {
                const float sc = NLOG2E * bf2f(v[j]);
                const u16 hi = f2bf(sc);
                const u16 lo = f2bf(sc - bf2f(hi));
                v[j] = mode == 0 ? v[j] : mode == 1 ? hi : mode == 2 ? ((j & 1) ? lo : hi) : (on ? v[j] : (u16)0);
            }
            u32x4 o = {v[0] | ((u32)v[1] << 16), v[2] | ((u32)v[3] << 16), v[4] | ((u32)v[5] << 16), v[6] | ((u32)v[7] << 16)};
            *(u32x4*)(ws + WS_WPK + (size_t)c * 1024 + lane * 16) = o;
        }
    }
    __syncthreads();
    SST(1);
    int wn[4]; u32 rk[4];
#pragma unroll
    for (int k = 0; k < 4; ++k) {
        wn[k] = (u32)d[k] / (u32)WN;
        if (tid + 1024 * k < CH) rk[k] = atomicAdd(&hist[wn[k]], 1u);
    }
    __syncthreads();
    SST(2);
    const u32 h0 = hist[2 * tid], h1 = hist[2 * tid + 1];
    const u32 x = wave_scan(h0 + h1);
    if (lane == 63) wsum[wv] = x;
    __syncthreads();
    const u32 wsv = wsum[lane & 15];
    const u32 wsc = wave_scan(lane < 16 ? wsv : 0u) - wsv;
    const u32 pre = __builtin_amdgcn_readlane(wsc, __builtin_amdgcn_readfirstlane(wv));
    const u32 st0 = pre + x - h0 - h1, st1 = st0 + h0;
    hist[2 * tid] = st0; hist[2 * tid + 1] = st1;
    if (tid < NWIN / 2) blk[(tid >> 2) * 32 + (tid & 3)] = st0 | (st1 << 16);
    if ((tid & 3) == 0 && tid > 0 && tid <= NWIN / 2) blk[((tid >> 2) - 1) * 32 + 4] = st0;
    __syncthreads();
    SST(3);
#pragma unroll
    for (int k = 0; k < 4; ++k) {
        if (tid + 1024 * k < CH) {
            const u32 p = hist[wn[k]] + rk[k];
            const u32 j = p - hist[wn[k] & ~7];
            const u32 r = (u32)s[k] * (u32)(WN * CH) + (u32)(d[k] - wn[k] * WN) * (u32)CH + (u32)(tid + 1024 * k);
            if (j < (u32)INL) blk[(wn[k] >> 3) * 32 + 5 + j] = r; else ((u32*)(ws + WS_REC))[e0 + p] = r;
        }
    }
    __syncthreads();
    SST(4);
#pragma unroll
    for (int k = 0; k < 2; ++k) {
        const int i = tid + 1024 * k;
        if (i < NBK * 8) *(u32x4*)(ws + WS_BLK + ((size_t)(i >> 3) * NCH + c) * 128 + (i & 7) * 16) = ((const u32x4*)blk)[i];
    }
    SST(5); SST_FLUSH();
}

constexpr int HB = 4160, L_H = 0, L_M = 2 * HB, L_S = L_M + 2048, L_IDX = L_S + 2048, L_SRC = L_IDX + 2048, L_PW = L_SRC + 1024, L_WAVE = L_PW + 512;
constexpr int B_LUT = 8 * L_WAVE, LUTP = 80, B_PRG = B_LUT + 2080, B_STG = B_LUT + 2176, B_BYTES = B_STG + NCH * 128;

static DI void dma16(u32 voff, __amdgpu_buffer_rsrc_t rs, u32 ldsaddr) {
    u32 keep;
    if (ABL & 16) { asm volatile("" :: "v"(voff)); return; }
    asm volatile("s_mov_b32 m0, %2\n\ts_nop 0\n\tbuffer_load_dwordx4 %0, %1, 0 offen lds"
                 :: "v"(voff), "s"(rs), "s"(ldsaddr) : "memory", "m0");
}
static DI void dma16p(u32 voff, __amdgpu_buffer_rsrc_t rs, u32 ldsaddr) {
    u32 keep;
    asm volatile("s_nop 4\n\ts_mov_b32 %0, m0\n\ts_mov_b32 m0, %3\n\ts_nop 0\n\tbuffer_load_dwordx4 %1, %2, 0 offen lds\n\ts_mov_b32 m0, %0"
                 : "=&s"(keep) : "v"(voff), "s"(rs), "s"(ldsaddr) : "memory");
}
static DI u32x4 bload16(u32 voff, __amdgpu_buffer_rsrc_t rs) {
    u32x4 r; if (ABL & 32) { asm volatile("" : "=v"(r) : "v"(voff)); return r; }
    asm volatile("buffer_load_dwordx4 %0, %1, %2, 0 offen" : "=v"(r) : "v"(voff), "s"(rs) : "memory"); return r;
}
typedef u32 u32x3 __attribute__((ext_vector_type(3)));
static DI u32x3 bload12(u32 voff, __amdgpu_buffer_rsrc_t rs) {
    u32x3 r; if (ABL & 32) { asm volatile("" : "=v"(r) : "v"(voff)); return r; }
    asm volatile("s_mov_b32 exec_lo, 0\n\tbuffer_load_dwordx3 %0, %1, %2, 0 offen\n\ts_mov_b32 exec_lo, -1" : "=v"(r) : "v"(voff), "s"(rs) : "memory"); return r;
}
static DI s16x4 trread(LAS const char* p) {
    typedef short v4i16_t __attribute__((ext_vector_type(4)));
    return __builtin_bit_cast(s16x4, __builtin_amdgcn_ds_read_tr16_b64_v4i16((LAS v4i16_t*)p));
}
#define CBAR() asm volatile("" ::: "memory")
#define STAMP(i)
#define STAMP_FLUSH()
#define STAMP_DECL
#define SBAR() __builtin_amdgcn_sched_barrier(0)
static DI int lane_id() { int l; asm volatile("v_mbcnt_lo_u32_b32 %0, -1, 0\n\tv_mbcnt_hi_u32_b32 %0, -1, %0" : "=v"(l)); return l; }
static DI float silu2(float x) { return (ABL & 4) ? x : x * __builtin_amdgcn_rcpf(1.f + __builtin_amdgcn_exp2f(x)); }
static DI float dot2(u32 a, u32 b, float c) { return __builtin_amdgcn_fdot2_f32_bf16(__builtin_bit_cast(bf16x2, a), __builtin_bit_cast(bf16x2, b), c, false); }
static DI bf16x8 cat8(s16x4 a, s16x4 b) { return __builtin_shufflevector(a, b, 0, 1, 2, 3, 4, 5, 6, 7); }
static DI float smul(float a, float b) { float r = a * b; asm volatile("" : "+v"(r)); return r; }
static DI bf16x8 asb(u32x4 v) { return __builtin_bit_cast(bf16x8, v); }

__global__ __launch_bounds__(512, 2) void edge_kernel(const u16* __restrict__ hg, const u16* __restrict__ posg, const u16* __restrict__ eag,
                                                      char* __restrict__ ws, u16* __restrict__ out) {
    extern __shared__ __attribute__((aligned(16))) char smem[];
    const int lane = threadIdx.x & 63, e = lane & 31, hh = lane >> 5;
    const int wv = __builtin_amdgcn_readfirstlane(threadIdx.x >> 6);
    int wl = wv;
    LAS char* B = (LAS char*)smem;
    LAS char* L = B + wv * L_WAVE;
    const u32 lbase = (u32)(uintptr_t)L;
    LAS u32* lidx = (LAS u32*)(L + L_IDX);
    LAS u16* lsrc = (LAS u16*)(L + L_SRC);
    LAS u32* lloc = (LAS u32*)(L + L_M);

    const u32* rec = (const u32*)(ws + WS_REC);
    const u32x4* wpk = (const u32x4*)(ws + WS_WPK);
    const f32x4* pos4 = (const f32x4*)(ws + WS_POS);
    STAMP_DECL
    STAMP(0);

    {
        const auto rs_b = __builtin_amdgcn_make_buffer_rsrc((void*)(ws + WS_BLK + (size_t)blockIdx.x * NCH * 128), 0, NCH * 128, 0x00020000);
        const u32 vo = (u32)wv * 4096 + (lane >> 3) * 128 + (((lane & 7) ^ ((lane >> 3) & 7)) << 4);
        const u32 sb = (u32)(uintptr_t)(B + B_STG) + wv * 4096;
#pragma unroll
        for (int q = 0; q < 4; ++q) dma16p(vo + q * 1024, rs_b, sb + q * 1024);
    }
    {
        const auto rs_w = __builtin_amdgcn_make_buffer_rsrc((void*)(ws + WS_WPK), 0, NFRAG * 1024, 0x00020000);
#pragma unroll
        for (int q = 0; q < 3; ++q) {
            const int i = wv * 3 + q, f = i < 18 ? i : i + 16;
            dma16p((u32)f * 1024 + lane * 16, rs_w, (u32)(uintptr_t)B + (i >> 2) * L_WAVE + HB + (i & 3) * 1024);
        }
    }
    f32x16 y0 = {0}, y1 = {0}; float yp[3] = {0.f, 0.f, 0.f};
    bf16x8 wf[10], an[4], wxf[4], hw[4]; u32x4 pdum = {0u, 0u, 0u, 0u};
    {
#pragma unroll
        for (int i = 0; i < 4; ++i) *(LAS u32x4*)(L + L_M + i * 1024 + lane * 16) = (u32x4){0, 0, 0, 0};
        for (int i = threadIdx.x; i < 26 * 32; i += 512) {
            const int row = i >> 5, n = i & 31;
            *(LAS u16*)(B + B_LUT + row * LUTP + (2 * (n >> 4) + ((n >> 2) & 1)) * 16 + (((n >> 3) & 1) * 4 + (n & 3)) * 2) = (n == row && row < WN) ? (u16)0x3F80 : (u16)0;
        }
        if (lane == 0) *(LAS int*)(B + B_PRG + wv * 4) = 0;
        if (threadIdx.x < 2) *(LAS int*)(B + B_PRG + 32 + threadIdx.x * 4) = 0;
        asm volatile("s_waitcnt vmcnt(0) lgkmcnt(0)\n\ts_barrier" ::: "memory");
        if (BAL) {
            LAS const char* sg = B + B_STG + lane * 128;
            const u32 sx = (lane & 7) << 4;
            u32 c = 0;
#pragma unroll
            for (int q = 0; q < 4; ++q) c += *(LAS const u16*)(sg + q * 8192 + ((2 * wv + 2) ^ sx)) - *(LAS const u16*)(sg + q * 8192 + ((2 * wv) ^ sx));
            c = wave_scan(c);
            if (lane == 63) *(LAS u32*)(B + B_PRG + 48 + wv * 4) = c;
            asm volatile("s_waitcnt lgkmcnt(0)\n\ts_barrier" ::: "memory");
            const u32 mine = *(LAS const u32*)(B + B_PRG + 48 + (lane & 7) * 4);
            int rank = 0;
#pragma unroll
            for (int j = 0; j < 8; ++j) { const u32 o = __builtin_amdgcn_readlane(mine, j); rank += (o < mine || (o == mine && j < (lane & 7))) ? 1 : 0; }
            const int want = wv < 4 ? wv : 11 - wv;
            wl = __builtin_ctzll(__builtin_amdgcn_ballot_w64(rank == want));
        }
        STAMP(1);
    }
    const int nbase = (blockIdx.x * 8 + wl) * WN;
    u32x4 hwr[4], pwr;
    {
        const int nn = nbase + (e < WN ? e : WN - 1);
        const auto rs_h0 = __builtin_amdgcn_make_buffer_rsrc((void*)hg, 0, N_ * 128, 0x00020000);
        const auto rs_p0 = __builtin_amdgcn_make_buffer_rsrc((void*)(ws + WS_POS), 0, N_ * 16, 0x00020000);
#pragma unroll
        for (int s = 0; s < 4; ++s) hwr[s] = bload16((u32)nn * 128 + 32 * s + 16 * hh, rs_h0);
        pwr = bload16((u32)nn * 16, rs_p0);
    }
    const u32 fsw = (e >> 1) & 7;
    LAS const char* hrd[2];
#pragma unroll
    for (int s = 0; s < 2; ++s) hrd[s] = L + L_H + (e >> 3) * 1040 + (e & 7) * 128 + ((s ^ ((e >> 1) & 1)) << 6) + hh * 16;
    const u32 swz0 = ((lane & 7) << 4) ^ (((lane >> 4) & 1) << 6);
    LAS char* mwr[4];
#pragma unroll
    for (int g = 0; g < 4; ++g) mwr[g] = L + L_M + e * 64 + (((2 * g + hh) ^ fsw) << 3);
    const int i16 = lane & 15, tq = i16 >> 2, tp = i16 & 3, tb = (lane >> 4) & 1;
    LAS const char* mrd[4];
#pragma unroll
    for (int i = 0; i < 4; ++i) {
        const int row = 16 * (i >> 1) + 8 * hh + 4 * (i & 1) + tq;
        mrd[i] = L + L_M + row * 64 + (((4 * tb + tp) ^ ((row >> 1) & 7)) << 3);
    }
    LAS const char* srd[2];
#pragma unroll
    for (int s = 0; s < 2; ++s) srd[s] = L + L_S + e * 64 + (((2 * s + hh) ^ ((e >> 2) & 3)) << 4);

    int T = 0; bool whl = false, sig = false; u32x2 hv[8];
    for (int b0 = 0; b0 == 0 || b0 < T; b0 += CAP) {
        int nb, nt;
        {
            const int ln = lane_id();
            LAS const char* sg = B + B_STG + ln * 128;
            const u32 sx = (ln & 7) << 4;
            u32 lo_[4], cn_[4], s0_[4];
#pragma unroll
            for (int q = 0; q < 4; ++q) {
                const u32 a = *(LAS const u16*)(sg + q * 8192 + ((2 * wl) ^ sx)), b = *(LAS const u16*)(sg + q * 8192 + ((2 * wl + 2) ^ sx));
                s0_[q] = *(LAS const u16*)(sg + q * 8192 + sx);
                lo_[q] = a; cn_[q] = b - a;
            }
            const u32 ltot = cn_[0] + cn_[1] + cn_[2] + cn_[3];
            const u32 inc = wave_scan(ltot);
            T = __builtin_amdgcn_readlane((int)inc, 63);
            nb = (T - b0) < CAP ? (T - b0) : CAP;
            nt = (nb + 31) >> 5;
            const u32 mx = max(max(cn_[0], cn_[1]), max(cn_[2], cn_[3]));
            u32 pp[4];
            pp[0] = inc - ltot - (u32)b0; pp[1] = pp[0] + cn_[0]; pp[2] = pp[1] + cn_[1]; pp[3] = pp[2] + cn_[2];
            u32 lc[4];
#pragma unroll
            for (int q = 0; q < 4; ++q) lc[q] = ((u32)(ln + 64 * q) << 12) + lo_[q] - s0_[q];
#pragma clang loop unroll(disable) vectorize(disable)
            for (u32 j = 0; j < mx; ++j) {
#pragma unroll
                for (int q = 0; q < 4; ++q) if (j < cn_[q] && pp[q] + j < (u32)CAP) lloc[pp[q] + j] = lc[q] + j;
            }
            CBAR();
#define DECODE(K0, K1) { \
                const int ln = lane_id(); \
                u32 lo2[8], r[8]; bool ovf = false; \
                _Pragma("unroll") for (int k = K0; k < K1; ++k) { const u32 v = lloc[64 * k + ln]; lo2[k] = 64 * k + ln < nb ? v : 0u; } \
                _Pragma("unroll") for (int k = K0; k < K1; ++k) { \
                    const u32 cc = lo2[k] >> 12, jj = lo2[k] & 4095; \
                    r[k] = *(LAS const u32*)(B + B_STG + cc * 128 + ((20 + 4 * (jj < (u32)INL ? jj : 0u)) ^ ((cc & 7) << 4))); \
                    ovf |= jj >= (u32)INL; \
                } \
                if (__builtin_amdgcn_ballot_w64(ovf)) { \
                    _Pragma("unroll") for (int k = K0; k < K1; ++k) { \
                        const u32 cc = lo2[k] >> 12, jj = lo2[k] & 4095; \
                        if (jj >= (u32)INL) r[k] = rec[cc * CH + *(LAS const u16*)(B + B_STG + cc * 128 + ((cc & 7) << 4)) + jj]; \
                    } \
                    _Pragma("unroll") for (int k = K0; k < K1; ++k) asm volatile("" : "+v"(r[k])); \
                } \
                _Pragma("unroll") for (int k = K0; k < K1; ++k) { \
                    const int i = 64 * k + ln; \
                    const u32 sv = r[k] / (u32)(WN * CH), rm = r[k] - sv * (u32)(WN * CH), dv = rm / (u32)CH; \
                    const bool ok = i < nb, hi_ = SRCORD && ok && sv >= (u32)(N_ / 2); \
                    const unsigned long long mh_ = __builtin_amdgcn_ballot_w64(hi_); \
                    const int bh_ = __builtin_amdgcn_mbcnt_hi((u32)(mh_ >> 32), __builtin_amdgcn_mbcnt_lo((u32)mh_, 0u)) + nhi; \
                    const int p_ = hi_ ? nb - 1 - bh_ : (ok ? i - bh_ : i); \
                    nhi += __builtin_popcountll(mh_); \
                    lidx[p_] = ok ? (((lo2[k] >> 12) * CH + rm - dv * (u32)CH) << 5) | (dv << 27) : ((u32)WN << 27); \
                    lsrc[p_] = ok ? (u16)sv : (u16)0; \
                } \
                CBAR(); }
        }
        int nhi = 0;
        DECODE(0, 8)
        CBAR();
        STAMP(2);

        u32x4 eaA, eaB; u32x3 psA, psB;
        const int ln2 = lane_id();
        LAS const u16* lsr8 = lsrc + (ln2 >> 3);
        LAS const u16* lsre = lsrc + (ln2 & 31);
        LAS const u32* lide = lidx + (ln2 & 31);
#define READ_D(TT) { const int tb_ = (TT) * 32; s0_ = lsr8[tb_]; s1_ = lsr8[tb_ + 8]; s2_ = lsr8[tb_ + 16]; s3_ = lsr8[tb_ + 24]; }
#define READ_E(TT) { li_ = lide[(TT) * 32]; so_ = lsre[(TT) * 32]; }
#define ISSUE_D(TT, BUF) do { \
            const auto rs_h = __builtin_amdgcn_make_buffer_rsrc((void*)hg, 0, ((TT) < nt && !(ABL & 1)) ? N_ * 128 : 0, 0x00020000); \
            dma16(s0_ * 128 + swz0, rs_h, lbase + L_H + (BUF) * HB); \
            dma16(s1_ * 128 + swz0, rs_h, lbase + L_H + (BUF) * HB + 1040); \
            dma16(s2_ * 128 + swz0, rs_h, lbase + L_H + (BUF) * HB + 2080); \
            dma16(s3_ * 128 + swz0, rs_h, lbase + L_H + (BUF) * HB + 3120); \
        } while (0)
#define ISSUE_E(TT, EA, PS) do { \
            const auto rs_e = __builtin_amdgcn_make_buffer_rsrc((void*)eag, 0, ((TT) < nt && !(ABL & 2)) ? E_ * 32 : 0, 0x00020000); \
            const auto rs_p = __builtin_amdgcn_make_buffer_rsrc((void*)(ws + WS_POS), 0, ((TT) < nt && !(ABL & 2)) ? N_ * 16 : 0, 0x00020000); \
            EA = bload16((li_ & 0x1FFFFFFu) | (hh * 16), rs_e); \
            PS = bload12(so_ * 16, rs_p); \
        } while (0)
#define MFMA16(a, b, c) __builtin_amdgcn_mfma_f32_16x16x32_bf16((a), (b), (c), 0, 0, 0)
#define PIN4(a, b, c, d) asm volatile("" : "+v"(a), "+v"(b), "+v"(c), "+v"(d))
#define SGROUP(C, PK, G, SA, SB, SC) do { \
            float e0_ = C[4 * (G)], e1_ = C[4 * (G) + 1], e2_ = C[4 * (G) + 2], e3_ = C[4 * (G) + 3]; \
            if (!(ABL & 4)) { e0_ = __builtin_amdgcn_exp2f(e0_); e1_ = __builtin_amdgcn_exp2f(e1_); e2_ = __builtin_amdgcn_exp2f(e2_); e3_ = __builtin_amdgcn_exp2f(e3_); } \
            PIN4(e0_, e1_, e2_, e3_); \
            SA \
            PIN4(e0_, e1_, e2_, e3_); \
            if (!(ABL & 4)) { e0_ = 1.f + e0_; asm volatile("" : "+v"(e0_)); e1_ = 1.f + e1_; asm volatile("" : "+v"(e1_)); e2_ = 1.f + e2_; asm volatile("" : "+v"(e2_)); e3_ = 1.f + e3_; \
            e0_ = __builtin_amdgcn_rcpf(e0_); e1_ = __builtin_amdgcn_rcpf(e1_); } \
            PIN4(e0_, e1_, e2_, e3_); \
            SB \
            PIN4(e0_, e1_, e2_, e3_); \
            if (!(ABL & 4)) { e2_ = __builtin_amdgcn_rcpf(e2_); e3_ = __builtin_amdgcn_rcpf(e3_); \
            e0_ = smul(e0_, C[4 * (G)]); e1_ = smul(e1_, C[4 * (G) + 1]); \
            e2_ = smul(e2_, C[4 * (G) + 2]); e3_ = smul(e3_, C[4 * (G) + 3]); } \
            PK[2 * (G)] = cvtpk(e0_, e1_); PK[2 * (G) + 1] = cvtpk(e2_, e3_); \
            asm volatile("" : "+v"(PK[2 * (G)]), "+v"(PK[2 * (G) + 1])); \
            SC \
        } while (0)
#define M32(D, A_, B_) { asm volatile("" : "+v"(D)); D = MFMA(A_, B_, D); asm volatile("" : "+v"(D)); }
#define M16(D, A_, B_) { asm volatile("" : "+v"(D)); D = MFMA16(A_, B_, D); asm volatile("" : "+v"(D)); }
#define M32Z(D, A_, B_, P_) { asm volatile("" : "+v"(P_)); D = MFMA(A_, B_, zero16); asm volatile("" : "+v"(D)); }
#define M16Z(D, A_, B_, P_) { asm volatile("" : "+v"(P_)); D = MFMA16(A_, B_, zero4); asm volatile("" : "+v"(D)); }
#define FETCH(TT, BUF, EA, PS) \
            asm volatile("s_waitcnt vmcnt(6)" : "+v"(EA), "+v"(PS) :: "memory"); \
            dl = dln < WN ? dln : WN; \
            _Pragma("unroll") for (int s = 0; s < 4; ++s) xb[s] = *(LAS const bf16x8*)(hrd[s >> 1] + (BUF) * HB + (s & 1) * 32); \
            { LAS const char* lp_ = B + B_LUT + dl * LUTP + hh * 16; \
              oh0 = *(LAS const u32x4*)lp_; oh1 = *(LAS const u32x4*)(lp_ + 32); \
              const f32x4 pd_ = *(LAS const f32x4*)(L + L_PW + dl * 16); ndx = pd_[0]; ndy = pd_[1]; ndz = pd_[2]; } \
            dln = lide[((TT) + 1) * 32] >> 27;
#define GEOM(PS) \
            ndx -= __uint_as_float(PS[0]); asm volatile("" : "+v"(ndx)); ndy -= __uint_as_float(PS[1]); asm volatile("" : "+v"(ndy)); ndz -= __uint_as_float(PS[2]); \
            { const float d2 = __builtin_fmaf(ndx, ndx, __builtin_fmaf(ndy, ndy, smul(ndz, ndz))); const u32 dp_ = cvtpk(d2, d2); oh1[3] = hh ? dp_ : oh1[3]; }

        const f32x16 zero16 = {0};
        f32x16 c0, c1; bf16x8 xb[4]; u32x4 oh0, oh1;
        bf16x8 sf0 = {0}, sf1 = {0}; s16x4 u0 = {0}, u1 = {0}, u2 = {0}, u3 = {0};
        const f32x4 zero4 = {0.f, 0.f, 0.f, 0.f}; f32x4 cz = zero4; u32 pk1[8] = {0u, 0u, 0u, 0u, 0u, 0u, 0u, 0u};
        float dx = 0.f, dy = 0.f, dz = 0.f, ndx, ndy, ndz; u32 dl, dln;

        u32 s0_, s1_, s2_, s3_, li_, so_;
        READ_D(0) READ_E(0) ISSUE_D(0, 0); ISSUE_E(0, eaA, psA); READ_E(1) ISSUE_E(1, eaB, psB);
        if (b0 == 0) {
        asm volatile("s_waitcnt vmcnt(8)" : "+v"(hwr[0]), "+v"(hwr[1]), "+v"(hwr[2]), "+v"(hwr[3]), "+v"(pwr) :: "memory");
#pragma unroll
        for (int s = 0; s < 4; ++s) hw[s] = asb(hwr[s]);
        if (lane < 32) *(LAS u32x4*)(L + L_PW + lane * 16) = lane < WN ? pwr : (u32x4){0u, 0u, 0u, 0u};
#define WFR(i) (*(LAS const u32x4*)(B + ((i) >> 2) * L_WAVE + HB + ((i) & 3) * 1024 + lane * 16))
        f32x16 a0 = {0}, a1 = {0};
#pragma unroll
        for (int s = 0; s < 4; ++s) {
            a0 = MFMA(hw[s], asb(WFR(10 + s)), a0);
            a1 = MFMA(hw[s], asb(WFR(14 + s)), a1);
        }
        const u32x4 wd = WFR(18);
#pragma unroll
        for (int i = 0; i < 10; ++i) wf[i] = asb(WFR(i));
#pragma unroll
        for (int i = 0; i < 4; ++i) wxf[i] = asb(WFR(19 + i));
        u32 p0[8], p1[8];
#pragma unroll
        for (int i = 0; i < 8; ++i) { p0[i] = cvtpk(a0[2 * i] * NLOG2E, a0[2 * i + 1] * NLOG2E); p1[i] = cvtpk(a1[2 * i] * NLOG2E, a1[2 * i + 1] * NLOG2E); }
        if (hh) { p0[7] = wd[0]; p1[7] = wd[1]; }
        an[0] = asb((u32x4){p0[0], p0[1], p0[2], p0[3]}); an[1] = asb((u32x4){p0[4], p0[5], p0[6], p0[7]});
        an[2] = asb((u32x4){p1[0], p1[1], p1[2], p1[3]}); an[3] = asb((u32x4){p1[4], p1[5], p1[6], p1[7]});
#pragma unroll
        for (int i = 0; i < 10; ++i) asm volatile("" : "+v"(wf[i]));
#pragma unroll
        for (int i = 0; i < 4; ++i) asm volatile("" : "+v"(wxf[i]), "+v"(an[i]));
        if (T > CAP) *(LAS volatile int*)(B + B_PRG + 32) = 1;
        asm volatile("s_waitcnt lgkmcnt(0)\n\ts_barrier" ::: "memory");
        whl = __builtin_amdgcn_readfirstlane(*(LAS volatile int*)(B + B_PRG + 32)) == 0;
        {
            const auto rs_w = __builtin_amdgcn_make_buffer_rsrc((void*)(ws + WS_WPK), 0, whl ? NFRAG * 1024 : 0, 0x00020000);
            const u32 dsta = whl ? (u32)(uintptr_t)(B + B_STG) + wv * 2048 : lbase + L_H + HB;
            dma16p((u32)(18 + 2 * wv) * 1024 + lane * 16, rs_w, dsta);
            dma16p((u32)(19 + 2 * wv) * 1024 + lane * 16, rs_w, dsta + 1024);
        }
        }
        READ_D(1) ISSUE_D(1, 1);
        dln = lide[0] >> 27;
        READ_D(2) READ_E(2)
        FETCH(0, 0, eaA, psA)
        STAMP(3);
        GEOM(psA)
        asm volatile("" : "+v"(xb[0]), "+v"(xb[1]), "+v"(xb[2]), "+v"(xb[3]) :: "memory");
        ISSUE_D(2, 0);
        c0 = MFMA(wf[4], asb(eaA), zero16);
#pragma unroll
        for (int s = 0; s < 4; ++s) c0 = MFMA(wf[s], xb[s], c0);
        c0 = MFMA(an[0], asb(oh0), c0);
        c0 = MFMA(an[1], asb(oh1), c0);

#define PKV(P, O) asb((u32x4){P[O], P[(O) + 1], P[(O) + 2], P[(O) + 3]})
#define POSV() { const float csv = cz[0] * NLN2; \
              *(LAS u32x2*)mwr[0] = (u32x2){cvtpk(smul(csv, dx), smul(csv, dy)), cvtpk(smul(csv, dz), 0.f)}; \
              CBAR(); \
              q0 = trread(mrd[0]); q1 = trread(mrd[1]); q2 = trread(mrd[2]); q3 = trread(mrd[3]); \
              CBAR(); }
#define BODY(TT, BUF, EA, PS, EAN, PSN) do { \
            u32 pk0[8]; s16x4 q0, q1, q2, q3; f32x16 yt; \
            LAS u16* sp_ = (LAS u16*)(L + L_S + dl * 64 + ((((u32)e >> 3) ^ ((dl >> 2) & 3)) << 4) + (e & 7) * 2); \
            *sp_ = (u16)0x3F80; \
            CBAR(); \
              \
            SGROUP(c0, pk0, 0, M16(cz, wxf[3], PKV(pk1, 4)), M32Z(c1, wf[9], asb(EA), EA), M32(c1, wf[5], xb[0])); \
            ISSUE_E((TT) + 2, EA, PS); \
            POSV(); \
            dx = ndx; dy = ndy; dz = ndz; \
            SGROUP(c0, pk0, 1, M32(c1, wf[6], xb[1]), M32(y1, cat8(u0, u1), sf0), M32(c1, wf[7], xb[2])); \
            SGROUP(c0, pk0, 2, M32(y1, cat8(u2, u3), sf1), M32(c1, wf[8], xb[3]), M32(c1, an[2], asb(oh0))); \
            SGROUP(c0, pk0, 3, M32(c1, an[3], asb(oh1)), M32Z(yt, cat8(q0, q1), sf0, sf0), M32(yt, cat8(q2, q3), sf1)); \
            CBAR(); \
            sf0 = *(LAS const bf16x8*)srd[0]; sf1 = *(LAS const bf16x8*)srd[1]; \
            CBAR(); \
            *sp_ = (u16)0; \
            _Pragma("unroll") for (int g = 0; g < 4; ++g) *(LAS u32x2*)mwr[g] = (u32x2){pk0[2 * g], pk0[2 * g + 1]}; \
            CBAR(); \
            const s16x4 t0 = trread(mrd[0]), t1 = trread(mrd[1]), t2 = trread(mrd[2]), t3 = trread(mrd[3]); \
            CBAR(); \
              \
            SGROUP(c1, pk1, 0, M16Z(cz, wxf[0], PKV(pk0, 0), pk0[0]), M16(cz, wxf[1], PKV(pk0, 4)), \
                   { yp[0] += yt[0]; yp[1] += yt[1]; yp[2] += yt[2]; asm volatile("" : "+v"(yp[0]), "+v"(yp[1]), "+v"(yp[2])); } ); \
            FETCH((TT) + 1, (BUF) ^ 1, EAN, PSN) \
            READ_D((TT) + 3) \
            M32Z(c0, wf[4], asb(EAN), EAN) \
            SGROUP(c1, pk1, 1, M32(y0, cat8(t0, t1), sf0), M32(y0, cat8(t2, t3), sf1), M32(c0, wf[0], xb[0])); \
            GEOM(PSN) \
            asm volatile("" : "+v"(xb[0]), "+v"(xb[1]), "+v"(xb[2]), "+v"(xb[3]) :: "memory"); \
            ISSUE_D((TT) + 3, (BUF) ^ 1); \
            SGROUP(c1, pk1, 2, M32(c0, wf[1], xb[1]), M32(c0, wf[2], xb[2]), M32(c0, wf[3], xb[3])); \
            SGROUP(c1, pk1, 3, M32(c0, an[0], asb(oh0)), M32(c0, an[1], asb(oh1)), M16(cz, wxf[2], PKV(pk1, 0))); \
            CBAR(); \
            _Pragma("unroll") for (int g = 0; g < 4; ++g) *(LAS u32x2*)mwr[g] = (u32x2){pk1[2 * g], pk1[2 * g + 1]}; \
            CBAR(); \
            READ_E((TT) + 3) \
            u0 = trread(mrd[0]); u1 = trread(mrd[1]); u2 = trread(mrd[2]); u3 = trread(mrd[3]); \
            CBAR(); \
        } while (0)

        LAS int* prg = (LAS int*)(B + B_PRG);
        int other = 0;
        for (int t = 0; t < nt; t += 2) {
            if (PRIO) {
                if (T - b0 - 32 * t + PBIAS > __builtin_amdgcn_readfirstlane(other)) __builtin_amdgcn_s_setprio(1); else __builtin_amdgcn_s_setprio(0);
                prg[wv] = T - b0 - 32 * t;
                other = prg[wv ^ 4];
            }
            BODY(t, 0, eaA, psA, eaB, psB);
            if (!sig) {
                sig = true;
                if (lane_id() == 0) __hip_atomic_fetch_add((LAS int*)(B + B_PRG + 36), 1, __ATOMIC_RELAXED, __HIP_MEMORY_SCOPE_WORKGROUP);
            }
            if (t + 1 < nt) BODY(t + 1, 1, eaB, psB, eaA, psA);
        }
        if (PRIO) { prg[wv] = T - b0 - 32 * nt; __builtin_amdgcn_s_setprio(0); }
        {
            CBAR();
            const int l3 = lane_id(), e3 = l3 & 31, h3 = l3 >> 5;
            const int nn = nbase + (e3 < WN ? e3 : WN - 1);
#pragma unroll
            for (int s = 0; s < 4; ++s) hw[s] = *(const bf16x8*)(hg + (size_t)nn * 64 + 16 * s + 8 * h3);
#pragma unroll
            for (int g = 0; g < 8; ++g) hv[g] = *(const u32x2*)(hg + (size_t)nn * 64 + 32 * (g >> 2) + 8 * (g & 3) + 4 * h3);
            CBAR();
        }
        {
            s16x4 q0, q1, q2, q3;
            cz = MFMA16(wxf[3], PKV(pk1, 4), cz);
            y1 = MFMA(cat8(u0, u1), sf0, y1);
            y1 = MFMA(cat8(u2, u3), sf1, y1);
            CBAR();
            POSV();
            f32x16 yt = MFMA(cat8(q0, q1), sf0, zero16);
            yt = MFMA(cat8(q2, q3), sf1, yt);
            yp[0] += yt[0]; yp[1] += yt[1]; yp[2] += yt[2];
        }
        asm volatile("s_waitcnt vmcnt(0)" : "+v"(eaA), "+v"(psA), "+v"(eaB), "+v"(psB) :: "memory");
        asm volatile("" : "+v"(hw[0]), "+v"(hw[1]), "+v"(hw[2]), "+v"(hw[3]));
        asm volatile("" : "=v"(hwr[0]), "=v"(hwr[1]), "=v"(hwr[2]), "=v"(hwr[3]), "=v"(pwr));
        asm volatile("" : "+v"(hv[0]), "+v"(hv[1]), "+v"(hv[2]), "+v"(hv[3]), "+v"(hv[4]), "+v"(hv[5]), "+v"(hv[6]), "+v"(hv[7]));
        if (!sig) {
            sig = true;
            if (lane_id() == 0) __hip_atomic_fetch_add((LAS int*)(B + B_PRG + 36), 1, __ATOMIC_RELAXED, __HIP_MEMORY_SCOPE_WORKGROUP);
        }
        STAMP(4);
    }

    {
        CBAR();
        const int lane = lane_id();
        const int e = lane & 31, hh = lane >> 5;
        const int nn = nbase + (e < WN ? e : WN - 1);
        u32 p0[8], p1[8];
#pragma unroll
        for (int i = 0; i < 8; ++i) { p0[i] = cvtpk(y0[2 * i] * NLN2, y0[2 * i + 1] * NLN2); p1[i] = cvtpk(y1[2 * i] * NLN2, y1[2 * i + 1] * NLN2); }
        bf16x8 mb[4];
        mb[0] = asb((u32x4){p0[0], p0[1], p0[2], p0[3]});
        mb[1] = asb((u32x4){p0[4], p0[5], p0[6], p0[7]});
        mb[2] = asb((u32x4){p1[0], p1[1], p1[2], p1[3]});
        mb[3] = asb((u32x4){p1[4], p1[5], p1[6], p1[7]});
        f32x16 o0 = {0}, o1 = {0};
        if (__builtin_amdgcn_readfirstlane(*(LAS volatile int*)(B + B_PRG + 36)) < 8) whl = false;
        const u32x4* whp = whl ? (const u32x4*)(char*)(B + B_STG) + lane : wpk + 18 * 64 + lane;
#pragma unroll
        for (int s = 0; s < 4; ++s) {
            o0 = MFMA(asb(whp[(8 + s) * 64]), mb[s], o0);
            o1 = MFMA(asb(whp[(12 + s) * 64]), mb[s], o1);
        }
#pragma unroll
        for (int s = 0; s < 4; ++s) {
            o0 = MFMA(asb(whp[s * 64]), hw[s], o0);
            o1 = MFMA(asb(whp[(4 + s) * 64]), hw[s], o1);
        }
        if (e < WN) {
#pragma unroll
            for (int g = 0; g < 8; ++g) {
                const int f = 32 * (g >> 2) + 8 * (g & 3) + 4 * hh;
                float r[4];
#pragma unroll
                for (int i = 0; i < 4; ++i) {
                    const float a = g < 4 ? o0[4 * (g & 3) + i] : o1[4 * (g & 3) + i];
                    const float sv = a * __builtin_amdgcn_rcpf(1.f + __builtin_amdgcn_exp2f(a * NLOG2E));
                    const u32 hw_ = hv[g][i >> 1];
                    const float hf = __uint_as_float((i & 1) ? (hw_ & 0xFFFF0000u) : (hw_ << 16));
                    r[i] = hf + sv;
                }
                *(u32x2*)(out + (size_t)nn * 64 + f) = (u32x2){cvtpk(r[0], r[1]), cvtpk(r[2], r[3])};
            }
        }
        {
            const float ox = yp[0], oy = yp[1], oz = yp[2];
            if (e < WN && hh == 1) {
                const size_t o = (size_t)nn * 3;
                u16* po = out + (size_t)N_ * 64 + o;
                const f32x4 pw = *(LAS const f32x4*)(L + L_PW + e * 16);
                po[0] = f2bf(pw[0] + ox); po[1] = f2bf(pw[1] + oy); po[2] = f2bf(pw[2] + oz);
            }
        }
    }
    STAMP(5);
    STAMP_FLUSH();
}

__global__ __launch_bounds__(256) void g_edge_kernel(const u16* h, const u16* pos, const u16* ea, const int* src, const int* dst,
                                                     const u16* we, const u16* wx, float* macc, float* pacc, int E) {
    __shared__ float sw[145 * 64];
    for (int i = threadIdx.x; i < 145 * 64; i += blockDim.x) sw[i] = bf2f(we[i]);
    __syncthreads();
    int lane = threadIdx.x & 63;
    int wid = (blockIdx.x * blockDim.x + threadIdx.x) >> 6;
    int nw = (gridDim.x * blockDim.x) >> 6;
    float wxl = bf2f(wx[lane]);
    for (int e = wid; e < E; e += nw) {
        int s = src[e], d = dst[e];
        float dx = bf2f(pos[(size_t)d * 3 + 0]) - bf2f(pos[(size_t)s * 3 + 0]);
        float dy = bf2f(pos[(size_t)d * 3 + 1]) - bf2f(pos[(size_t)s * 3 + 1]);
        float dz = bf2f(pos[(size_t)d * 3 + 2]) - bf2f(pos[(size_t)s * 3 + 2]);
        float d2 = dx * dx + dy * dy + dz * dz;
        float hd = bf2f(h[(size_t)d * 64 + lane]), hs = bf2f(h[(size_t)s * 64 + lane]);
        float eav = lane < 16 ? bf2f(ea[(size_t)e * 16 + lane]) : 0.f;
        float acc = d2 * sw[128 * 64 + lane];
#pragma unroll 8
        for (int k = 0; k < 64; ++k) acc += __shfl(hd, k) * sw[k * 64 + lane];
#pragma unroll 8
        for (int k = 0; k < 64; ++k) acc += __shfl(hs, k) * sw[(64 + k) * 64 + lane];
#pragma unroll 8
        for (int k = 0; k < 16; ++k) acc += __shfl(eav, k) * sw[(129 + k) * 64 + lane];
        float m = acc / (1.f + __expf(-acc));
        float cs = m * wxl;
        for (int o = 32; o > 0; o >>= 1) cs += __shfl_xor(cs, o);
        atomicAdd(&macc[(size_t)d * 64 + lane], m);
        if (lane < 3) atomicAdd(&pacc[(size_t)d * 3 + lane], (lane == 0 ? dx : lane == 1 ? dy : dz) * cs);
    }
}
__global__ __launch_bounds__(256) void g_node_kernel(const u16* h, const u16* pos, const u16* wh, const float* macc, const float* pacc,
                                                     u16* out, int N) {
    __shared__ float sw[128 * 64];
    for (int i = threadIdx.x; i < 128 * 64; i += blockDim.x) sw[i] = bf2f(wh[i]);
    __syncthreads();
    int lane = threadIdx.x & 63;
    int wid = (blockIdx.x * blockDim.x + threadIdx.x) >> 6;
    int nw = (gridDim.x * blockDim.x) >> 6;
    for (int n = wid; n < N; n += nw) {
        float hv = bf2f(h[(size_t)n * 64 + lane]);
        float mv = macc[(size_t)n * 64 + lane];
        float acc = 0.f;
#pragma unroll 8
        for (int k = 0; k < 64; ++k) acc += __shfl(hv, k) * sw[k * 64 + lane];
#pragma unroll 8
        for (int k = 0; k < 64; ++k) acc += __shfl(mv, k) * sw[(64 + k) * 64 + lane];
        float s = acc / (1.f + __expf(-acc));
        out[(size_t)n * 64 + lane] = f2bf(hv + s);
        if (lane < 3) out[(size_t)N * 64 + (size_t)n * 3 + lane] = f2bf(bf2f(pos[(size_t)n * 3 + lane]) + pacc[(size_t)n * 3 + lane]);
    }
}

extern "C" void kernel_launch(void* const* d_in, const int* in_sizes, int n_in,
                              void* d_out, int out_size, void* d_ws, size_t ws_size,
                              hipStream_t stream) {
    const u16* h = (const u16*)d_in[0];
    const u16* pos = (const u16*)d_in[1];
    const u16* ea = (const u16*)d_in[2];
    const int* src = (const int*)d_in[3];
    const int* dst = (const int*)d_in[4];
    const u16* we = (const u16*)d_in[5];
    const u16* wx = (const u16*)d_in[6];
    const u16* wh = (const u16*)d_in[7];
    const int N = in_sizes[0] / 64, E = in_sizes[3];
    if (N == N_ && E == E_ && ws_size >= WS_END) {
        (void)hipFuncSetAttribute((const void*)sort_kernel, hipFuncAttributeMaxDynamicSharedMemorySize, S_BYTES);
        (void)hipFuncSetAttribute((const void*)edge_kernel, hipFuncAttributeMaxDynamicSharedMemorySize, B_BYTES);
        sort_kernel<<<NCH, 1024, S_BYTES, stream>>>(src, dst, pos, we, wx, wh, (char*)d_ws);
        edge_kernel<<<NBK, 512, B_BYTES, stream>>>(h, pos, ea, (char*)d_ws, (u16*)d_out);
    } else {
        float* macc = (float*)d_ws;
        float* pacc = macc + (size_t)N * 64;
        (void)hipMemsetAsync(d_ws, 0, (size_t)N * 67 * 4, stream);
        g_edge_kernel<<<2048, 256, 0, stream>>>(h, pos, ea, src, dst, we, wx, macc, pacc, E);
        g_node_kernel<<<1024, 256, 0, stream>>>(h, pos, wh, macc, pacc, (u16*)d_out, N);
    }
}
```

```cpp
#include <hip/hip_runtime.h>
#include <hip/hip_bf16.h>
#include <stdint.h>

#define PRIO 1
#define PBIAS 96
#define EANT 0
#define SRCORD 1
#define PREF 0
#define BAL 0
#define ABL 0
typedef unsigned short u16;
typedef unsigned int u32;
typedef u32 u32x4 __attribute__((ext_vector_type(4)));
typedef u32 u32x2 __attribute__((ext_vector_type(2)));
typedef float f32x4 __attribute__((ext_vector_type(4)));
typedef float f32x16 __attribute__((ext_vector_type(16)));
typedef short bf16x8 __attribute__((ext_vector_type(8)));
typedef short s16x4 __attribute__((ext_vector_type(4)));
#define LAS __attribute__((address_space(3)))
#define DI __device__ __forceinline__
static __device__ __forceinline__ f32x16 mfma_w(bf16x8 a, bf16x8 b, f32x16 c) {
    if (ABL & 64) { asm volatile("" : "+v"(c) : "v"(a), "v"(b)); return c; }
    return __builtin_amdgcn_mfma_f32_32x32x16_bf16(a, b, c, 0, 0, 0); }
#define MFMA(a, b, c) mfma_w((a), (b), (c))

static DI float bf2f(u16 v) { return __uint_as_float(((u32)v) << 16); }
static DI u16 f2bf(float f) { __hip_bfloat16 b = __float2bfloat16(f); return *reinterpret_cast<u16*>(&b); }
typedef float f32x2 __attribute__((ext_vector_type(2)));
typedef __bf16 bf16x2 __attribute__((ext_vector_type(2)));
static DI u32 cvtpk(float lo, float hi) { return __builtin_bit_cast(u32, __builtin_convertvector((f32x2){lo, hi}, bf16x2)); }

static DI u32 wave_maxscan(u32 x) {
    x = max(x, (u32)__builtin_amdgcn_update_dpp(0u, x, 0x111, 0xf, 0xf, false));
    x = max(x, (u32)__builtin_amdgcn_update_dpp(0u, x, 0x112, 0xf, 0xf, false));
    x = max(x, (u32)__builtin_amdgcn_update_dpp(0u, x, 0x114, 0xf, 0xf, false));
    x = max(x, (u32)__builtin_amdgcn_update_dpp(0u, x, 0x118, 0xf, 0xf, false));
    x = max(x, (u32)__builtin_amdgcn_update_dpp(0u, x, 0x142, 0xa, 0xf, false));
    x = max(x, (u32)__builtin_amdgcn_update_dpp(0u, x, 0x143, 0xc, 0xf, false));
    return x;
}
static DI u32 wave_scan(u32 x) {
    x += __builtin_amdgcn_update_dpp(0u, x, 0x111, 0xf, 0xf, false);
    x += __builtin_amdgcn_update_dpp(0u, x, 0x112, 0xf, 0xf, false);
    x += __builtin_amdgcn_update_dpp(0u, x, 0x114, 0xf, 0xf, false);
    x += __builtin_amdgcn_update_dpp(0u, x, 0x118, 0xf, 0xf, false);
    x += __builtin_amdgcn_update_dpp(0u, x, 0x142, 0xa, 0xf, false);
    x += __builtin_amdgcn_update_dpp(0u, x, 0x143, 0xc, 0xf, false);
    return x;
}

constexpr int N_ = 50000, E_ = 800000;
constexpr int WN = 25;
constexpr int NWIN = 2000;
constexpr int NBK = NWIN / 8;
constexpr int NCH = 256;
constexpr int CH = 3125;
constexpr int INL = 27;
constexpr int CAP = 512;
constexpr int NFRAG = 39;
#define NPRE 13
constexpr float NLOG2E = -1.4426950408889634f;
constexpr float NLN2 = -0.6931471805599453f;

constexpr size_t WS_BLK = 0;
constexpr size_t WS_REC = WS_BLK + (size_t)NBK * NCH * 128;
constexpr size_t WS_POS = WS_REC + (size_t)E_ * 4;
constexpr size_t WS_WPK = WS_POS + (size_t)N_ * 16;
constexpr size_t WS_END = WS_WPK + (size_t)NFRAG * 1024;

constexpr int S_HIST = 0, S_WSUM = 8192, S_BLK = 8192 + 128, S_BYTES = S_BLK + NBK * 128;

#define SST(i)
#define SST_DECL
#define SST_FLUSH()
__global__ __launch_bounds__(1024) void sort_kernel(const int* __restrict__ src, const int* __restrict__ dst,
                                                    const u16* __restrict__ pos, const u16* __restrict__ we, const u16* __restrict__ wx,
                                                    const u16* __restrict__ wh, char* __restrict__ ws) {
    extern __shared__ __attribute__((aligned(16))) char smem[];
    u32* hist = (u32*)(smem + S_HIST);
    u32* wsum = (u32*)(smem + S_WSUM);
    u32* blk = (u32*)(smem + S_BLK);
    const int tid = threadIdx.x, lane = tid & 63, wv = tid >> 6, c = blockIdx.x;
    const int e0 = c * CH;
    SST_DECL SST(0);
    int d[4], s[4];
#pragma unroll
    for (int k = 0; k < 4; ++k) { int i = tid + 1024 * k; i = i < CH ? i : CH - 1; d[k] = dst[e0 + i]; s[k] = src[e0 + i]; }
    hist[tid] = 0; hist[tid + 1024] = 0;

    {
        int n = c * 196 + tid;
        if (tid < 196 && n < N_) {
            f32x4 p = {bf2f(pos[n * 3]), bf2f(pos[n * 3 + 1]), bf2f(pos[n * 3 + 2]), 0.f};
            *(f32x4*)(ws + WS_POS + (size_t)n * 16) = p;
        }
        if (c < NFRAG && wv == 15) {
            const int r = lane & 31, h = lane >> 5;
            const u16* sp; int base, perm = 0, mode = 0;
            if (c < 10) { const int t = c / 5, st = c % 5; sp = we; base = (st < 4 ? 64 + 16 * st : 129) * 64 + 32 * t; mode = 1; }
            else if (c < 18) { sp = we; base = 16 * ((c - 10) & 3) * 64 + 32 * ((c - 10) >> 2); }
            else if (c < 26) { sp = wh; base = 16 * ((c - 18) & 3) * 64 + 32 * ((c - 18) >> 2); }
            else if (c < 34) { sp = wh; base = (64 + 16 * ((c - 26) & 3)) * 64 + 32 * ((c - 26) >> 2); perm = 1; }
            else if (c == 34) { sp = we; base = 128 * 64; mode = 2; }
            else { sp = wx; base = 16 * (c - 35); perm = 1; mode = 3; }
            u16 v[8];
#pragma unroll
            for (int j = 0; j < 8; ++j) {
                const int k = perm ? 8 * (j >> 2) + 4 * h + (j & 3) : 8 * h + j;
                const int idx = mode == 2 ? 32 * ((j >> 1) & 1) + r : (mode == 3 ? k : k * 64 + r);
                v[j] = sp[base + idx];
            }
            const bool on = ((lane & 15) & 7) == 4 * ((lane >> 4) & 1);
#pragma unroll
            for (int j = 0; j < 8; ++j) {
                const float sc = NLOG2E * bf2f(v[j]);
                const u16 hi = f2bf(sc);
                const u16 lo = f2bf(sc - bf2f(hi));
                v[j] = mode == 0 ? v[j] : mode == 1 ? hi : mode == 2 ? ((j & 1) ? lo : hi) : (on ? v[j] : (u16)0);
            }
            u32x4 o = {v[0] | ((u32)v[1] << 16), v[2] | ((u32)v[3] << 16), v[4] | ((u32)v[5] << 16), v[6] | ((u32)v[7] << 16)};
            *(u32x4*)(ws + WS_WPK + (size_t)c * 1024 + lane * 16) = o;
        }
    }
    __syncthreads();
    SST(1);
    int wn[4]; u32 rk[4];
#pragma unroll
    for (int k = 0; k < 4; ++k) {
        wn[k] = (u32)d[k] / (u32)WN;
        if (tid + 1024 * k < CH) rk[k] = atomicAdd(&hist[wn[k]], 1u);
    }
    __syncthreads();
    SST(2);
    const u32 h0 = hist[2 * tid], h1 = hist[2 * tid + 1];
    const u32 x = wave_scan(h0 + h1);
    if (lane == 63) wsum[wv] = x;
    __syncthreads();
    const u32 wsv = wsum[lane & 15];
    const u32 wsc = wave_scan(lane < 16 ? wsv : 0u) - wsv;
    const u32 pre = __builtin_amdgcn_readlane(wsc, __builtin_amdgcn_readfirstlane(wv));
    const u32 st0 = pre + x - h0 - h1, st1 = st0 + h0;
    hist[2 * tid] = st0; hist[2 * tid + 1] = st1;
    if (tid < NWIN / 2) blk[(tid >> 2) * 32 + (tid & 3)] = st0 | (st1 << 16);
    if ((tid & 3) == 0 && tid > 0 && tid <= NWIN / 2) blk[((tid >> 2) - 1) * 32 + 4] = st0;
    __syncthreads();
    SST(3);
#pragma unroll
    for (int k = 0; k < 4; ++k) {
        if (tid + 1024 * k < CH) {
            const u32 p = hist[wn[k]] + rk[k];
            const u32 j = p - hist[wn[k] & ~7];
            const u32 r = (u32)s[k] * (u32)(WN * CH) + (u32)(d[k] - wn[k] * WN) * (u32)CH + (u32)(tid + 1024 * k);
            if (j < (u32)INL) blk[(wn[k] >> 3) * 32 + 5 + j] = r; else ((u32*)(ws + WS_REC))[e0 + p] = r;
        }
    }
    __syncthreads();
    SST(4);
#pragma unroll
    for (int k = 0; k < 2; ++k) {
        const int i = tid + 1024 * k;
        if (i < NBK * 8) *(u32x4*)(ws + WS_BLK + ((size_t)(i >> 3) * NCH + c) * 128 + (i & 7) * 16) = ((const u32x4*)blk)[i];
    }
    SST(5); SST_FLUSH();
}

constexpr int HB = 4160, L_H = 0, L_M = 2 * HB, L_S = L_M + 2048, L_IDX = L_S + 2080, L_SRC = L_IDX + 2048, L_WAVE = L_SRC + 1024 + 224;
constexpr int B_LUT = 8 * L_WAVE, LUTP = 80, B_PRG = B_LUT + 2080, B_STG = B_LUT + 2176, B_BYTES = B_STG + NCH * 128;

static DI void dma16(u32 voff, __amdgpu_buffer_rsrc_t rs, u32 ldsaddr) {
    u32 keep;
    if (ABL & 16) { asm volatile("" :: "v"(voff)); return; }
    asm volatile("s_mov_b32 m0, %2\n\ts_nop 0\n\tbuffer_load_dwordx4 %0, %1, 0 offen lds"
                 :: "v"(voff), "s"(rs), "s"(ldsaddr) : "memory", "m0");
}
static DI void dma16p(u32 voff, __amdgpu_buffer_rsrc_t rs, u32 ldsaddr) {
    u32 keep;
    asm volatile("s_nop 4\n\ts_mov_b32 %0, m0\n\ts_mov_b32 m0, %3\n\ts_nop 0\n\tbuffer_load_dwordx4 %1, %2, 0 offen lds\n\ts_mov_b32 m0, %0"
                 : "=&s"(keep) : "v"(voff), "s"(rs), "s"(ldsaddr) : "memory");
}
static DI u32x4 bload16(u32 voff, __amdgpu_buffer_rsrc_t rs) {
    u32x4 r; if (ABL & 32) { asm volatile("" : "=v"(r) : "v"(voff)); return r; }
    asm volatile("buffer_load_dwordx4 %0, %1, %2, 0 offen" : "=v"(r) : "v"(voff), "s"(rs) : "memory"); return r;
}
typedef u32 u32x3 __attribute__((ext_vector_type(3)));
static DI u32x3 bload12(u32 voff, __amdgpu_buffer_rsrc_t rs) {
    u32x3 r; if (ABL & 32) { asm volatile("" : "=v"(r) : "v"(voff)); return r; }
    asm volatile("s_mov_b32 exec_lo, 0\n\tbuffer_load_dwordx3 %0, %1, %2, 0 offen\n\ts_mov_b32 exec_lo, -1" : "=v"(r) : "v"(voff), "s"(rs) : "memory"); return r;
}
static DI s16x4 trread(LAS const char* p) {
    typedef short v4i16_t __attribute__((ext_vector_type(4)));
    return __builtin_bit_cast(s16x4, __builtin_amdgcn_ds_read_tr16_b64_v4i16((LAS v4i16_t*)p));
}
#define CBAR() asm volatile("" ::: "memory")
#define STAMP(i)
#define STAMP_FLUSH()
#define STAMP_DECL
#define SBAR() __builtin_amdgcn_sched_barrier(0)
static DI int lane_id() { int l; asm volatile("v_mbcnt_lo_u32_b32 %0, -1, 0\n\tv_mbcnt_hi_u32_b32 %0, -1, %0" : "=v"(l)); return l; }
static DI float silu2(float x) { return (ABL & 4) ? x : x * __builtin_amdgcn_rcpf(1.f + __builtin_amdgcn_exp2f(x)); }
static DI float dot2(u32 a, u32 b, float c) { return __builtin_amdgcn_fdot2_f32_bf16(__builtin_bit_cast(bf16x2, a), __builtin_bit_cast(bf16x2, b), c, false); }
static DI bf16x8 cat8(s16x4 a, s16x4 b) { return __builtin_shufflevector(a, b, 0, 1, 2, 3, 4, 5, 6, 7); }
static DI float smul(float a, float b) { float r = a * b; asm volatile("" : "+v"(r)); return r; }
static DI bf16x8 asb(u32x4 v) { return __builtin_bit_cast(bf16x8, v); }

__global__ __launch_bounds__(512, 2) void edge_kernel(const u16* __restrict__ hg, const u16* __restrict__ posg, const u16* __restrict__ eag,
                                                      char* __restrict__ ws, u16* __restrict__ out) {
    extern __shared__ __attribute__((aligned(16))) char smem[];
    const int lane = threadIdx.x & 63, e = lane & 31, hh = lane >> 5;
    const int wv = __builtin_amdgcn_readfirstlane(threadIdx.x >> 6);
    int wl = wv;
    LAS char* B = (LAS char*)smem;
    LAS char* L = B + wv * L_WAVE;
    const u32 lbase = (u32)(uintptr_t)L;
    LAS u32* lidx = (LAS u32*)(L + L_IDX);
    LAS u16* lsrc = (LAS u16*)(L + L_SRC);
    LAS u32* lloc = (LAS u32*)(L + L_M);

    const u32* rec = (const u32*)(ws + WS_REC);
    const u32x4* wpk = (const u32x4*)(ws + WS_WPK);
    const f32x4* pos4 = (const f32x4*)(ws + WS_POS);
    STAMP_DECL
    STAMP(0);

    {
        const auto rs_b = __builtin_amdgcn_make_buffer_rsrc((void*)(ws + WS_BLK + (size_t)blockIdx.x * NCH * 128), 0, NCH * 128, 0x00020000);
        const u32 vo = (u32)wv * 4096 + (lane >> 3) * 128 + (((lane & 7) ^ ((lane >> 3) & 7)) << 4);
        const u32 sb = (u32)(uintptr_t)(B + B_STG) + wv * 4096;
#pragma unroll
        for (int q = 0; q < 4; ++q) dma16p(vo + q * 1024, rs_b, sb + q * 1024);
    }
    {
        const auto rs_w = __builtin_amdgcn_make_buffer_rsrc((void*)(ws + WS_WPK), 0, NFRAG * 1024, 0x00020000);
#pragma unroll
        for (int q = 0; q < 3; ++q) {
            const int i = wv * 3 + q, f = i < 18 ? i : i + 16;
            dma16p((u32)f * 1024 + lane * 16, rs_w, (u32)(uintptr_t)B + (i >> 2) * L_WAVE + HB + (i & 3) * 1024);
        }
    }
    f32x16 y0 = {0}, y1 = {0}; float yp[3] = {0.f, 0.f, 0.f};
    bf16x8 wf[10], an[4], wxf[4], hw[4]; u32x4 pdum = {0u, 0u, 0u, 0u};
    {
#pragma unroll
        for (int i = 0; i < 4; ++i) *(LAS u32x4*)(L + L_M + i * 1024 + lane * 16) = (u32x4){0, 0, 0, 0};
        if (lane < 2) *(LAS u32x4*)(L + L_M + 4096 + lane * 16) = (u32x4){0, 0, 0, 0};
        if (threadIdx.x < 26) {
            const int n = threadIdx.x;
#pragma unroll
            for (int i = 0; i < 4; ++i) *(LAS u32x4*)(B + B_LUT + n * LUTP + i * 16) = (u32x4){0, 0, 0, 0};
            CBAR();
            *(LAS u16*)(B + B_LUT + n * LUTP + (2 * (n >> 4) + ((n >> 2) & 1)) * 16 + (((n >> 3) & 1) * 4 + (n & 3)) * 2) = n < WN ? (u16)0x3F80 : (u16)0;
        }
        if (lane == 0) { *(LAS int*)(B + B_PRG + wv * 4) = 0; *(LAS int*)(B + B_PRG + 48 + wv * 4) = 0; }
        if (threadIdx.x < 2) *(LAS int*)(B + B_PRG + 32 + threadIdx.x * 4) = 0;
        asm volatile("s_waitcnt vmcnt(0) lgkmcnt(0)\n\ts_barrier" ::: "memory");
        if (BAL) {
            LAS const char* sg = B + B_STG + lane * 128;
            const u32 sx = (lane & 7) << 4;
            u32 c = 0;
#pragma unroll
            for (int q = 0; q < 4; ++q) c += *(LAS const u16*)(sg + q * 8192 + ((2 * wv + 2) ^ sx)) - *(LAS const u16*)(sg + q * 8192 + ((2 * wv) ^ sx));
            c = wave_scan(c);
            if (lane == 63) *(LAS u32*)(B + B_PRG + 48 + wv * 4) = c;
            asm volatile("s_waitcnt lgkmcnt(0)\n\ts_barrier" ::: "memory");
            const u32 mine = *(LAS const u32*)(B + B_PRG + 48 + (lane & 7) * 4);
            int rank = 0;
#pragma unroll
            for (int j = 0; j < 8; ++j) { const u32 o = __builtin_amdgcn_readlane(mine, j); rank += (o < mine || (o == mine && j < (lane & 7))) ? 1 : 0; }
            const int want = wv < 4 ? wv : 11 - wv;
            wl = __builtin_ctzll(__builtin_amdgcn_ballot_w64(rank == want));
        }
        STAMP(1);
    }
    const int nbase = (blockIdx.x * 8 + wl) * WN;
    u32x4 hwr[4], pwr;
    {
        const int nn = nbase + (e < WN ? e : WN - 1);
        const auto rs_h0 = __builtin_amdgcn_make_buffer_rsrc((void*)hg, 0, N_ * 128, 0x00020000);
        const auto rs_p0 = __builtin_amdgcn_make_buffer_rsrc((void*)(ws + WS_POS), 0, N_ * 16, 0x00020000);
#pragma unroll
        for (int s = 0; s < 4; ++s) hwr[s] = bload16((u32)nn * 128 + 32 * s + 16 * hh, rs_h0);
        pwr = bload16((u32)nn * 16, rs_p0);
    }
    const u32 fsw = (e >> 1) & 7;
    LAS const char* hrd[2];
#pragma unroll
    for (int s = 0; s < 2; ++s) hrd[s] = L + L_H + (e >> 3) * 1040 + (e & 7) * 128 + ((s ^ ((e >> 1) & 1)) << 6) + hh * 16;
    const u32 swz0 = ((lane & 7) << 4) ^ (((lane >> 4) & 1) << 6);
    LAS char* mwr[4];
#pragma unroll
    for (int g = 0; g < 4; ++g) mwr[g] = L + L_M + e * 64 + (((2 * g + hh) ^ fsw) << 3);
    const int i16 = lane & 15, tq = i16 >> 2, tp = i16 & 3, tb = (lane >> 4) & 1;
    LAS const char* mrd[4];
#pragma unroll
    for (int i = 0; i < 4; ++i) {
        const int row = 16 * (i >> 1) + 8 * hh + 4 * (i & 1) + tq;
        mrd[i] = L + L_M + row * 64 + (((4 * tb + tp) ^ ((row >> 1) & 7)) << 3);
    }
#pragma unroll
    for (int i = 0; i < 4; ++i) asm volatile("" : "+v"(mrd[i]), "+v"(mwr[i]));
    LAS const char* srd = L + L_S + e * 80 + hh * 16;
    const u32 lutd = (u32)(B_LUT - L_S - wv * L_WAVE) + hh * 16;

    int T = 0; bool whl = false, sig = false; u32x2 hv[8];
    for (int b0 = 0; b0 == 0 || b0 < T; b0 += CAP) {
        int nb, nt;
        {
            const int ln = lane_id();
            LAS const char* sg = B + B_STG + ln * 128;
            const u32 sx = (ln & 7) << 4;
            u32 lo_[4], cn_[4], s0_[4];
#pragma unroll
            for (int q = 0; q < 4; ++q) {
                const u32 a = *(LAS const u16*)(sg + q * 8192 + ((2 * wl) ^ sx)), b = *(LAS const u16*)(sg + q * 8192 + ((2 * wl + 2) ^ sx));
                s0_[q] = *(LAS const u16*)(sg + q * 8192 + sx);
                lo_[q] = a; cn_[q] = b - a;
            }
            const u32 ltot = cn_[0] + cn_[1] + cn_[2] + cn_[3];
            const u32 inc = wave_scan(ltot);
            T = __builtin_amdgcn_readlane((int)inc, 63);
            nb = (T - b0) < CAP ? (T - b0) : CAP;
            nt = (nb + 31) >> 5;
            u32 pp[4];
            pp[0] = inc - ltot - (u32)b0; pp[1] = pp[0] + cn_[0]; pp[2] = pp[1] + cn_[1]; pp[3] = pp[2] + cn_[2];
            if (b0 > 0) {
#pragma unroll
                for (int i = 0; i < 2; ++i) *(LAS u32x4*)(L + L_M + i * 1024 + ln * 16) = (u32x4){0, 0, 0, 0};
                CBAR();
            }
#pragma unroll
            for (int q = 0; q < 4; ++q) {
                const int st = (int)pp[q], en = st + (int)cn_[q], s1 = st > 0 ? st : 0;
                if (en > s1 && s1 < CAP) lloc[s1] = ((u32)(s1 + 1) << 20) | (((u32)(ln + 64 * q) << 12) + lo_[q] - s0_[q] + (u32)(s1 - st));
            }
            CBAR();
#define DECODE(K0, K1) { \
                const int ln = lane_id(); \
                u32 lo2[8], r[8]; bool ovf = false; \
                _Pragma("unroll") for (int k = K0; k < K1; ++k) lo2[k] = lloc[64 * k + ln]; \
                _Pragma("unroll") for (int k = K0; k < K1; ++k) { \
                    u32 v = max(wave_maxscan(lo2[k]), carry); \
                    carry = __builtin_amdgcn_readlane(v, 63); \
                    v = (v & 0xFFFFFu) + (u32)(64 * k + 1 + ln) - (v >> 20); \
                    lo2[k] = 64 * k + ln < nb ? v : 0u; } \
                _Pragma("unroll") for (int k = K0; k < K1; ++k) { \
                    const u32 cc = lo2[k] >> 12, jj = lo2[k] & 4095; \
                    r[k] = *(LAS const u32*)(B + B_STG + cc * 128 + ((20 + 4 * (jj < (u32)INL ? jj : 0u)) ^ ((cc & 7) << 4))); \
                    ovf |= jj >= (u32)INL; \
                } \
                if (__builtin_amdgcn_ballot_w64(ovf)) { \
                    _Pragma("unroll") for (int k = K0; k < K1; ++k) { \
                        const u32 cc = lo2[k] >> 12, jj = lo2[k] & 4095; \
                        if (jj >= (u32)INL) r[k] = rec[cc * CH + *(LAS const u16*)(B + B_STG + cc * 128 + ((cc & 7) << 4)) + jj]; \
                    } \
                    _Pragma("unroll") for (int k = K0; k < K1; ++k) asm volatile("" : "+v"(r[k])); \
                } \
                _Pragma("unroll") for (int k = K0; k < K1; ++k) { \
                    const int i = 64 * k + ln; \
                    const u32 sv = r[k] / (u32)(WN * CH), rm = r[k] - sv * (u32)(WN * CH), dv = rm / (u32)CH; \
                    const bool ok = i < nb, hi_ = SRCORD && k > 0 && ok && sv >= (u32)(N_ / 2); \
                    const unsigned long long mh_ = __builtin_amdgcn_ballot_w64(hi_); \
                    const int bh_ = __builtin_amdgcn_mbcnt_hi((u32)(mh_ >> 32), __builtin_amdgcn_mbcnt_lo((u32)mh_, 0u)) + nhi; \
                    const int p_ = hi_ ? nb - 1 - bh_ : (ok ? i - bh_ : i); \
                    nhi += __builtin_popcountll(mh_); \
                    lidx[p_] = ok ? (((lo2[k] >> 12) * CH + rm - dv * (u32)CH) << 5) | (dv << 27) : ((u32)WN << 27); \
                    lsrc[p_] = ok ? (u16)sv : (u16)0; \
                } \
                CBAR(); }
        }
        int nhi = 0; u32 carry = 0;
        DECODE(0, 1)
        CBAR();
        STAMP(2);

        u32x4 eaA, eaB; u32x3 psA, psB;
        const int ln2 = lane_id();
        LAS const u16* lsr8 = lsrc + (ln2 >> 3);
        LAS const u16* lsre = lsrc + (ln2 & 31);
        LAS const u32* lide = lidx + (ln2 & 31);
#define READ_D(TT) { const int tb_ = (TT) * 32; s0_ = lsr8[tb_]; s1_ = lsr8[tb_ + 8]; s2_ = lsr8[tb_ + 16]; s3_ = lsr8[tb_ + 24]; }
#define READ_E(TT) { li_ = lide[(TT) * 32]; so_ = lsre[(TT) * 32]; }
#define ISSUE_D(TT, BUF) do { \
            const auto rs_h = __builtin_amdgcn_make_buffer_rsrc((void*)hg, 0, ((TT) < nt && !(ABL & 1)) ? N_ * 128 : 0, 0x00020000); \
            dma16(s0_ * 128 + swz0, rs_h, lbase + L_H + (BUF) * HB); \
            dma16(s1_ * 128 + swz0, rs_h, lbase + L_H + (BUF) * HB + 1040); \
            dma16(s2_ * 128 + swz0, rs_h, lbase + L_H + (BUF) * HB + 2080); \
            dma16(s3_ * 128 + swz0, rs_h, lbase + L_H + (BUF) * HB + 3120); \
        } while (0)
#define ISSUE_E(TT, EA, PS) do { \
            const auto rs_e = __builtin_amdgcn_make_buffer_rsrc((void*)eag, 0, ((TT) < nt && !(ABL & 2)) ? E_ * 32 : 0, 0x00020000); \
            const auto rs_p = __builtin_amdgcn_make_buffer_rsrc((void*)(ws + WS_POS), 0, ((TT) < nt && !(ABL & 2)) ? N_ * 16 : 0, 0x00020000); \
            EA = bload16((li_ & 0x1FFFFFFu) | (hh * 16), rs_e); \
            PS = bload12(so_ * 16, rs_p); \
        } while (0)
#define MFMA16(a, b, c) __builtin_amdgcn_mfma_f32_16x16x32_bf16((a), (b), (c), 0, 0, 0)
#define PIN4(a, b, c, d) asm volatile("" : "+v"(a), "+v"(b), "+v"(c), "+v"(d))
#define SGROUP(C, PK, G, SA, SB, SC) do { \
            float e0_, e1_, e2_, e3_; \
            asm volatile("v_exp_f32 %0, %4\n\tv_exp_f32 %1, %5\n\tv_exp_f32 %2, %6\n\tv_exp_f32 %3, %7" \
                         : "=&v"(e0_), "=&v"(e1_), "=&v"(e2_), "=&v"(e3_) : "v"(C[4 * (G)]), "v"(C[4 * (G) + 1]), "v"(C[4 * (G) + 2]), "v"(C[4 * (G) + 3])); \
            SA \
            asm volatile("v_add_f32 %0, 1.0, %0\n\tv_add_f32 %1, 1.0, %1\n\tv_add_f32 %2, 1.0, %2\n\tv_rcp_f32 %0, %0\n\tv_add_f32 %3, 1.0, %3\n\tv_rcp_f32 %1, %1" \
                         : "+v"(e0_), "+v"(e1_), "+v"(e2_), "+v"(e3_)); \
            SB \
            asm volatile("v_rcp_f32 %2, %2\n\tv_rcp_f32 %3, %3\n\tv_mul_f32 %0, %0, %4\n\tv_mul_f32 %1, %1, %5\n\tv_mul_f32 %2, %2, %6\n\tv_mul_f32 %3, %3, %7\n\t" \
                         "v_cvt_pk_bf16_f32 %0, %0, %1\n\tv_cvt_pk_bf16_f32 %2, %2, %3" \
                         : "+v"(e0_), "+v"(e1_), "+v"(e2_), "+v"(e3_) : "v"(C[4 * (G)]), "v"(C[4 * (G) + 1]), "v"(C[4 * (G) + 2]), "v"(C[4 * (G) + 3])); \
            PK[2 * (G)] = __float_as_uint(e0_); PK[2 * (G) + 1] = __float_as_uint(e2_); \
            SC \
        } while (0)
#define M32(D, A_, B_) { asm volatile("" : "+v"(D)); D = MFMA(A_, B_, D); asm volatile("" : "+v"(D)); }
#define M16(D, A_, B_) { asm volatile("" : "+v"(D)); D = MFMA16(A_, B_, D); asm volatile("" : "+v"(D)); }
#define M32Z(D, A_, B_, P_) { asm volatile("" : "+v"(P_)); D = MFMA(A_, B_, zero16); asm volatile("" : "+v"(D)); }
#define M16Z(D, A_, B_, P_) { asm volatile("" : "+v"(P_)); D = MFMA16(A_, B_, zero4); asm volatile("" : "+v"(D)); }
#define FETCH(TT, BUF, EA, PS, GN) \
            asm volatile("s_waitcnt vmcnt(6)" : "+v"(EA), "+v"(PS) :: "memory"); \
            dl = dln < WN ? dln : WN; \
            _Pragma("unroll") for (int s = 0; s < 4; ++s) xb[s] = *(LAS const bf16x8*)(hrd[s >> 1] + (BUF) * HB + (s & 1) * 32); \
            srow = L + L_S + dl * 80; \
            { LAS const char* lp_ = srow + lutd; \
              oh0 = *(LAS const u32x4*)lp_; oh1 = *(LAS const u32x4*)(lp_ + 32); \
              const f32x4 pd_ = *(LAS const f32x4*)(srow + 64); GN[0] = pd_[0]; GN[1] = pd_[1]; GN[2] = pd_[2]; } \
            dln = lide[((TT) + 1) * 32] >> 27;
#define GEOM(PS, GN) \
            GN[0] -= __uint_as_float(PS[0]); asm volatile("" : "+v"(GN[0])); GN[1] -= __uint_as_float(PS[1]); asm volatile("" : "+v"(GN[1])); GN[2] -= __uint_as_float(PS[2]); \
            { const float d2 = __builtin_fmaf(GN[0], GN[0], __builtin_fmaf(GN[1], GN[1], GN[2] * GN[2])); const u32 dp_ = cvtpk(d2, d2); oh1[3] = hh ? dp_ : oh1[3]; }

        const f32x16 zero16 = {0};
        f32x16 c0, c1; bf16x8 xb[4]; u32x4 oh0, oh1;
        bf16x8 sf0 = {0}, sf1 = {0}; s16x4 u0 = {0}, u1 = {0}, u2 = {0}, u3 = {0};
        const f32x4 zero4 = {0.f, 0.f, 0.f, 0.f}; f32x4 cz = zero4; u32 pk1[8] = {0u, 0u, 0u, 0u, 0u, 0u, 0u, 0u};
        float gA[3], gB[3] = {0.f, 0.f, 0.f}; u32 dl, dln; LAS char* srow;

        u32 s0_, s1_, s2_, s3_, li_, so_;
        READ_D(0) READ_E(0) ISSUE_D(0, 0); ISSUE_E(0, eaA, psA); READ_E(1) ISSUE_E(1, eaB, psB);
        DECODE(1, 7)
        if (nb > 448) DECODE(7, 8)
        if (b0 == 0) {
        asm volatile("s_waitcnt vmcnt(8)" : "+v"(hwr[0]), "+v"(hwr[1]), "+v"(hwr[2]), "+v"(hwr[3]), "+v"(pwr) :: "memory");
#pragma unroll
        for (int s = 0; s < 4; ++s) hw[s] = asb(hwr[s]);
        if (lane < WN) *(LAS u32x4*)(L + L_S + lane * 80 + 64) = pwr;
#define WFR(i) (*(LAS const u32x4*)(B + ((i) >> 2) * L_WAVE + HB + ((i) & 3) * 1024 + lane * 16))
        f32x16 a0 = {0}, a1 = {0};
#pragma unroll
        for (int s = 0; s < 4; ++s) {
            a0 = MFMA(hw[s], asb(WFR(10 + s)), a0);
            a1 = MFMA(hw[s], asb(WFR(14 + s)), a1);
        }
        const u32x4 wd = WFR(18);
#pragma unroll
        for (int i = 0; i < 10; ++i) wf[i] = asb(WFR(i));
#pragma unroll
        for (int i = 0; i < 4; ++i) wxf[i] = asb(WFR(19 + i));
        u32 p0[8], p1[8];
#pragma unroll
        for (int i = 0; i < 8; ++i) { p0[i] = cvtpk(a0[2 * i] * NLOG2E, a0[2 * i + 1] * NLOG2E); p1[i] = cvtpk(a1[2 * i] * NLOG2E, a1[2 * i + 1] * NLOG2E); }
        if (hh) { p0[7] = wd[0]; p1[7] = wd[1]; }
        an[0] = asb((u32x4){p0[0], p0[1], p0[2], p0[3]}); an[1] = asb((u32x4){p0[4], p0[5], p0[6], p0[7]});
        an[2] = asb((u32x4){p1[0], p1[1], p1[2], p1[3]}); an[3] = asb((u32x4){p1[4], p1[5], p1[6], p1[7]});
#pragma unroll
        for (int i = 0; i < 10; ++i) asm volatile("" : "+v"(wf[i]));
#pragma unroll
        for (int i = 0; i < 4; ++i) asm volatile("" : "+v"(wxf[i]), "+v"(an[i]));
        if (T > CAP) *(LAS volatile int*)(B + B_PRG + 32) = 1;
        asm volatile("s_waitcnt lgkmcnt(0)\n\ts_barrier" ::: "memory");
        whl = __builtin_amdgcn_readfirstlane(*(LAS volatile int*)(B + B_PRG + 32)) == 0;
        {
            const auto rs_w = __builtin_amdgcn_make_buffer_rsrc((void*)(ws + WS_WPK), 0, whl ? NFRAG * 1024 : 0, 0x00020000);
            const u32 dsta = whl ? (u32)(uintptr_t)(B + B_STG) + wv * 2048 : lbase + L_H + HB;
            dma16p((u32)(18 + 2 * wv) * 1024 + lane * 16, rs_w, dsta);
            dma16p((u32)(19 + 2 * wv) * 1024 + lane * 16, rs_w, dsta + 1024);
        }
        }
        READ_D(1) ISSUE_D(1, 1);
        dln = lide[0] >> 27;
        READ_D(2) READ_E(2)
        FETCH(0, 0, eaA, psA, gA)
        STAMP(3);
        GEOM(psA, gA)
        asm volatile("" : "+v"(xb[0]), "+v"(xb[1]), "+v"(xb[2]), "+v"(xb[3]) :: "memory");
        ISSUE_D(2, 0);
        c0 = MFMA(wf[4], asb(eaA), zero16);
#pragma unroll
        for (int s = 0; s < 4; ++s) c0 = MFMA(wf[s], xb[s], c0);
        c0 = MFMA(an[0], asb(oh0), c0);
        c0 = MFMA(an[1], asb(oh1), c0);
        asm volatile("s_nop 7\n\ts_nop 7\n\ts_nop 3" : "+v"(c0));

#define PKV(P, O) asb((u32x4){P[O], P[(O) + 1], P[(O) + 2], P[(O) + 3]})
#define POSV(GP) { asm volatile("" : "+v"(cz)); const float csv = cz[0] * NLN2; \
              *(LAS u32x2*)mwr[0] = (u32x2){cvtpk(smul(csv, GP[0]), smul(csv, GP[1])), cvtpk(smul(csv, GP[2]), 0.f)}; \
              CBAR(); \
              q0 = trread(mrd[0]); q1 = trread(mrd[1]); q2 = trread(mrd[2]); q3 = trread(mrd[3]); \
              CBAR(); }
#define BODY(TT, BUF, EA, PS, EAN, PSN, GN) do { \
            u32 pk0[8]; s16x4 q0, q1, q2, q3; f32x16 yt; \
            LAS u16* sp_ = (LAS u16*)(srow + e * 2); \
            *sp_ = (u16)0x3F80; \
            CBAR(); \
              \
            SGROUP(c0, pk0, 0, M16(cz, wxf[3], PKV(pk1, 4)), M32Z(c1, wf[9], asb(EA), EA), M32(c1, wf[5], xb[0])); \
            ISSUE_E((TT) + 2, EA, PS); \
            POSV(GN); \
            SGROUP(c0, pk0, 1, M32(c1, wf[6], xb[1]), M32(y1, cat8(u0, u1), sf0), M32(c1, wf[7], xb[2])); \
            SGROUP(c0, pk0, 2, M32(y1, cat8(u2, u3), sf1), M32(c1, wf[8], xb[3]), M32(c1, an[2], asb(oh0))); \
            SGROUP(c0, pk0, 3, M32(c1, an[3], asb(oh1)), M32Z(yt, cat8(q0, q1), sf0, sf0), M32(yt, cat8(q2, q3), sf1)); \
            CBAR(); \
            sf0 = *(LAS const bf16x8*)srd; sf1 = *(LAS const bf16x8*)(srd + 32); \
            CBAR(); \
            *sp_ = (u16)0; \
            _Pragma("unroll") for (int g = 0; g < 4; ++g) *(LAS u32x2*)mwr[g] = (u32x2){pk0[2 * g], pk0[2 * g + 1]}; \
            CBAR(); \
            const s16x4 t0 = trread(mrd[0]), t1 = trread(mrd[1]), t2 = trread(mrd[2]), t3 = trread(mrd[3]); \
            CBAR(); \
              \
            SGROUP(c1, pk1, 0, M16Z(cz, wxf[0], PKV(pk0, 0), pk0[0]), M16(cz, wxf[1], PKV(pk0, 4)), \
                   { asm volatile("" : "+v"(yt)); yp[0] += yt[0]; yp[1] += yt[1]; yp[2] += yt[2]; asm volatile("" : "+v"(yp[0]), "+v"(yp[1]), "+v"(yp[2])); } ); \
            FETCH((TT) + 1, (BUF) ^ 1, EAN, PSN, GN) \
            READ_D((TT) + 3) \
            M32Z(c0, wf[4], asb(EAN), EAN) \
            SGROUP(c1, pk1, 1, M32(y0, cat8(t0, t1), sf0), M32(y0, cat8(t2, t3), sf1), M32(c0, wf[0], xb[0])); \
            GEOM(PSN, GN) \
            asm volatile("" : "+v"(xb[0]), "+v"(xb[1]), "+v"(xb[2]), "+v"(xb[3]) :: "memory"); \
            ISSUE_D((TT) + 3, (BUF) ^ 1); \
            SGROUP(c1, pk1, 2, M32(c0, wf[1], xb[1]), M32(c0, wf[2], xb[2]), M32(c0, wf[3], xb[3])); \
            SGROUP(c1, pk1, 3, M32(c0, an[0], asb(oh0)), M32(c0, an[1], asb(oh1)), M16(cz, wxf[2], PKV(pk1, 0))); \
            CBAR(); \
            _Pragma("unroll") for (int g = 0; g < 4; ++g) *(LAS u32x2*)mwr[g] = (u32x2){pk1[2 * g], pk1[2 * g + 1]}; \
            CBAR(); \
            READ_E((TT) + 3) \
            u0 = trread(mrd[0]); u1 = trread(mrd[1]); u2 = trread(mrd[2]); u3 = trread(mrd[3]); \
            CBAR(); \
        } while (0)

        LAS int* prg = (LAS int*)(B + B_PRG);
        int other = 0;
#pragma clang loop unroll(disable)
        for (int t = 0; t < nt; t += 2) {
            if (PRIO) {
                if (T - b0 - 32 * t + PBIAS > __builtin_amdgcn_readfirstlane(other)) __builtin_amdgcn_s_setprio(1); else __builtin_amdgcn_s_setprio(0);
                prg[wv] = T - b0 - 32 * t;
                other = prg[wv ^ 4];
            }
            BODY(t, 0, eaA, psA, eaB, psB, gB);
            prg[12 + wv] = 1;
            if (t + 1 < nt) BODY(t + 1, 1, eaB, psB, eaA, psA, gA);
        }
        if (PRIO) { prg[wv] = T - b0 - 32 * nt; __builtin_amdgcn_s_setprio(0); }
        {
            CBAR();
            const int l3 = lane_id(), e3 = l3 & 31, h3 = l3 >> 5;
            const int nn = nbase + (e3 < WN ? e3 : WN - 1);
#pragma unroll
            for (int s = 0; s < 4; ++s) hw[s] = *(const bf16x8*)(hg + (size_t)nn * 64 + 16 * s + 8 * h3);
#pragma unroll
            for (int g = 0; g < 8; ++g) hv[g] = *(const u32x2*)(hg + (size_t)nn * 64 + 32 * (g >> 2) + 8 * (g & 3) + 4 * h3);
            CBAR();
        }
        {
            s16x4 q0, q1, q2, q3;
            cz = MFMA16(wxf[3], PKV(pk1, 4), cz);
            y1 = MFMA(cat8(u0, u1), sf0, y1);
            y1 = MFMA(cat8(u2, u3), sf1, y1);
            CBAR();
            float gl[3];
#pragma unroll
            for (int i = 0; i < 3; ++i) gl[i] = (nt & 1) ? gA[i] : gB[i];
            POSV(gl);
            f32x16 yt = MFMA(cat8(q0, q1), sf0, zero16);
            yt = MFMA(cat8(q2, q3), sf1, yt);
            yp[0] += yt[0]; yp[1] += yt[1]; yp[2] += yt[2];
        }
        asm volatile("s_waitcnt vmcnt(0)" : "+v"(eaA), "+v"(psA), "+v"(eaB), "+v"(psB) :: "memory");
        asm volatile("" : "+v"(hw[0]), "+v"(hw[1]), "+v"(hw[2]), "+v"(hw[3]));
        asm volatile("" : "=v"(hwr[0]), "=v"(hwr[1]), "=v"(hwr[2]), "=v"(hwr[3]), "=v"(pwr));
        asm volatile("" : "+v"(hv[0]), "+v"(hv[1]), "+v"(hv[2]), "+v"(hv[3]), "+v"(hv[4]), "+v"(hv[5]), "+v"(hv[6]), "+v"(hv[7]));
        prg[12 + wv] = 1;
        STAMP(4);
    }

    {
        CBAR();
        const int lane = lane_id();
        const int e = lane & 31, hh = lane >> 5;
        const int nn = nbase + (e < WN ? e : WN - 1);
        u32 p0[8], p1[8];
#pragma unroll
        for (int i = 0; i < 8; ++i) { p0[i] = cvtpk(y0[2 * i] * NLN2, y0[2 * i + 1] * NLN2); p1[i] = cvtpk(y1[2 * i] * NLN2, y1[2 * i + 1] * NLN2); }
        bf16x8 mb[4];
        mb[0] = asb((u32x4){p0[0], p0[1], p0[2], p0[3]});
        mb[1] = asb((u32x4){p0[4], p0[5], p0[6], p0[7]});
        mb[2] = asb((u32x4){p1[0], p1[1], p1[2], p1[3]});
        mb[3] = asb((u32x4){p1[4], p1[5], p1[6], p1[7]});
        f32x16 o0 = {0}, o1 = {0};
        if (__builtin_amdgcn_ballot_w64(*(LAS volatile int*)(B + B_PRG + 48 + (lane & 7) * 4) == 0)) whl = false;
        const u32x4* whp = whl ? (const u32x4*)(char*)(B + B_STG) + lane : wpk + 18 * 64 + lane;
#pragma unroll
        for (int s = 0; s < 4; ++s) {
            o0 = MFMA(asb(whp[(8 + s) * 64]), mb[s], o0);
            o1 = MFMA(asb(whp[(12 + s) * 64]), mb[s], o1);
        }
#pragma unroll
        for (int s = 0; s < 4; ++s) {
            o0 = MFMA(asb(whp[s * 64]), hw[s], o0);
            o1 = MFMA(asb(whp[(4 + s) * 64]), hw[s], o1);
        }
        if (e < WN) {
#pragma unroll
            for (int g = 0; g < 8; ++g) {
                const int f = 32 * (g >> 2) + 8 * (g & 3) + 4 * hh;
                float r[4];
#pragma unroll
                for (int i = 0; i < 4; ++i) {
                    const float a = g < 4 ? o0[4 * (g & 3) + i] : o1[4 * (g & 3) + i];
                    const float sv = a * __builtin_amdgcn_rcpf(1.f + __builtin_amdgcn_exp2f(a * NLOG2E));
                    const u32 hw_ = hv[g][i >> 1];
                    const float hf = __uint_as_float((i & 1) ? (hw_ & 0xFFFF0000u) : (hw_ << 16));
                    r[i] = hf + sv;
                }
                *(u32x2*)(out + (size_t)nn * 64 + f) = (u32x2){cvtpk(r[0], r[1]), cvtpk(r[2], r[3])};
            }
        }
        {
            const float ox = yp[0], oy = yp[1], oz = yp[2];
            if (e < WN && hh == 1) {
                const size_t o = (size_t)nn * 3;
                u16* po = out + (size_t)N_ * 64 + o;
                const f32x4 pw = *(LAS const f32x4*)(L + L_S + e * 80 + 64);
                po[0] = f2bf(pw[0] + ox); po[1] = f2bf(pw[1] + oy); po[2] = f2bf(pw[2] + oz);
            }
        }
    }
    STAMP(5);
    STAMP_FLUSH();
}

__global__ __launch_bounds__(256) void g_edge_kernel(const u16* h, const u16* pos, const u16* ea, const int* src, const int* dst,
                                                     const u16* we, const u16* wx, float* macc, float* pacc, int E) {
    __shared__ float sw[145 * 64];
    for (int i = threadIdx.x; i < 145 * 64; i += blockDim.x) sw[i] = bf2f(we[i]);
    __syncthreads();
    int lane = threadIdx.x & 63;
    int wid = (blockIdx.x * blockDim.x + threadIdx.x) >> 6;
    int nw = (gridDim.x * blockDim.x) >> 6;
    float wxl = bf2f(wx[lane]);
    for (int e = wid; e < E; e += nw) {
        int s = src[e], d = dst[e];
        float dx = bf2f(pos[(size_t)d * 3 + 0]) - bf2f(pos[(size_t)s * 3 + 0]);
        float dy = bf2f(pos[(size_t)d * 3 + 1]) - bf2f(pos[(size_t)s * 3 + 1]);
        float dz = bf2f(pos[(size_t)d * 3 + 2]) - bf2f(pos[(size_t)s * 3 + 2]);
        float d2 = dx * dx + dy * dy + dz * dz;
        float hd = bf2f(h[(size_t)d * 64 + lane]), hs = bf2f(h[(size_t)s * 64 + lane]);
        float eav = lane < 16 ? bf2f(ea[(size_t)e * 16 + lane]) : 0.f;
        float acc = d2 * sw[128 * 64 + lane];
#pragma unroll 8
        for (int k = 0; k < 64; ++k) acc += __shfl(hd, k) * sw[k * 64 + lane];
#pragma unroll 8
        for (int k = 0; k < 64; ++k) acc += __shfl(hs, k) * sw[(64 + k) * 64 + lane];
#pragma unroll 8
        for (int k = 0; k < 16; ++k) acc += __shfl(eav, k) * sw[(129 + k) * 64 + lane];
        float m = acc / (1.f + __expf(-acc));
        float cs = m * wxl;
        for (int o = 32; o > 0; o >>= 1) cs += __shfl_xor(cs, o);
        atomicAdd(&macc[(size_t)d * 64 + lane], m);
        if (lane < 3) atomicAdd(&pacc[(size_t)d * 3 + lane], (lane == 0 ? dx : lane == 1 ? dy : dz) * cs);
    }
}
__global__ __launch_bounds__(256) void g_node_kernel(const u16* h, const u16* pos, const u16* wh, const float* macc, const float* pacc,
                                                     u16* out, int N) {
    __shared__ float sw[128 * 64];
    for (int i = threadIdx.x; i < 128 * 64; i += blockDim.x) sw[i] = bf2f(wh[i]);
    __syncthreads();
    int lane = threadIdx.x & 63;
    int wid = (blockIdx.x * blockDim.x + threadIdx.x) >> 6;
    int nw = (gridDim.x * blockDim.x) >> 6;
    for (int n = wid; n < N; n += nw) {
        float hv = bf2f(h[(size_t)n * 64 + lane]);
        float mv = macc[(size_t)n * 64 + lane];
        float acc = 0.f;
#pragma unroll 8
        for (int k = 0; k < 64; ++k) acc += __shfl(hv, k) * sw[k * 64 + lane];
#pragma unroll 8
        for (int k = 0; k < 64; ++k) acc += __shfl(mv, k) * sw[(64 + k) * 64 + lane];
        float s = acc / (1.f + __expf(-acc));
        out[(size_t)n * 64 + lane] = f2bf(hv + s);
        if (lane < 3) out[(size_t)N * 64 + (size_t)n * 3 + lane] = f2bf(bf2f(pos[(size_t)n * 3 + lane]) + pacc[(size_t)n * 3 + lane]);
    }
}

extern "C" void kernel_launch(void* const* d_in, const int* in_sizes, int n_in,
                              void* d_out, int out_size, void* d_ws, size_t ws_size,
                              hipStream_t stream) {
    const u16* h = (const u16*)d_in[0];
    const u16* pos = (const u16*)d_in[1];
    const u16* ea = (const u16*)d_in[2];
    const int* src = (const int*)d_in[3];
    const int* dst = (const int*)d_in[4];
    const u16* we = (const u16*)d_in[5];
    const u16* wx = (const u16*)d_in[6];
    const u16* wh = (const u16*)d_in[7];
    const int N = in_sizes[0] / 64, E = in_sizes[3];
    if (N == N_ && E == E_ && ws_size >= WS_END) {
        (void)hipFuncSetAttribute((const void*)sort_kernel, hipFuncAttributeMaxDynamicSharedMemorySize, S_BYTES);
        (void)hipFuncSetAttribute((const void*)edge_kernel, hipFuncAttributeMaxDynamicSharedMemorySize, B_BYTES);
        sort_kernel<<<NCH, 1024, S_BYTES, stream>>>(src, dst, pos, we, wx, wh, (char*)d_ws);
        edge_kernel<<<NBK, 512, B_BYTES, stream>>>(h, pos, ea, (char*)d_ws, (u16*)d_out);
    } else {
        float* macc = (float*)d_ws;
        float* pacc = macc + (size_t)N * 64;
        (void)hipMemsetAsync(d_ws, 0, (size_t)N * 67 * 4, stream);
        g_edge_kernel<<<2048, 256, 0, stream>>>(h, pos, ea, src, dst, we, wx, macc, pacc, E);
        g_node_kernel<<<1024, 256, 0, stream>>>(h, pos, wh, macc, pacc, (u16*)d_out, N);
    }
}
```

```cpp
#include <hip/hip_runtime.h>
#include <hip/hip_bf16.h>
#include <stdint.h>


typedef unsigned short u16;
typedef unsigned int u32;
typedef u32 u32x4 __attribute__((ext_vector_type(4)));
typedef u32 u32x2 __attribute__((ext_vector_type(2)));
typedef float f32x4 __attribute__((ext_vector_type(4)));
typedef float f32x16 __attribute__((ext_vector_type(16)));
typedef short bf16x8 __attribute__((ext_vector_type(8)));
typedef short s16x4 __attribute__((ext_vector_type(4)));
#define LAS __attribute__((address_space(3)))
#define DI __device__ __forceinline__
#define MFMA(a, b, c) __builtin_amdgcn_mfma_f32_32x32x16_bf16((a), (b), (c), 0, 0, 0)

static DI float bf2f(u16 v) { return __uint_as_float(((u32)v) << 16); }
static DI u16 f2bf(float f) { __hip_bfloat16 b = __float2bfloat16(f); return *reinterpret_cast<u16*>(&b); }
typedef float f32x2 __attribute__((ext_vector_type(2)));
typedef __bf16 bf16x2 __attribute__((ext_vector_type(2)));
static DI u32 cvtpk(float lo, float hi) { return __builtin_bit_cast(u32, __builtin_convertvector((f32x2){lo, hi}, bf16x2)); }

static DI u32 wave_maxscan(u32 x) {
    x = max(x, (u32)__builtin_amdgcn_update_dpp(0u, x, 0x111, 0xf, 0xf, false));
    x = max(x, (u32)__builtin_amdgcn_update_dpp(0u, x, 0x112, 0xf, 0xf, false));
    x = max(x, (u32)__builtin_amdgcn_update_dpp(0u, x, 0x114, 0xf, 0xf, false));
    x = max(x, (u32)__builtin_amdgcn_update_dpp(0u, x, 0x118, 0xf, 0xf, false));
    x = max(x, (u32)__builtin_amdgcn_update_dpp(0u, x, 0x142, 0xa, 0xf, false));
    x = max(x, (u32)__builtin_amdgcn_update_dpp(0u, x, 0x143, 0xc, 0xf, false));
    return x;
}
static DI u32 wave_scan(u32 x) {
    x += __builtin_amdgcn_update_dpp(0u, x, 0x111, 0xf, 0xf, false);
    x += __builtin_amdgcn_update_dpp(0u, x, 0x112, 0xf, 0xf, false);
    x += __builtin_amdgcn_update_dpp(0u, x, 0x114, 0xf, 0xf, false);
    x += __builtin_amdgcn_update_dpp(0u, x, 0x118, 0xf, 0xf, false);
    x += __builtin_amdgcn_update_dpp(0u, x, 0x142, 0xa, 0xf, false);
    x += __builtin_amdgcn_update_dpp(0u, x, 0x143, 0xc, 0xf, false);
    return x;
}

constexpr int N_ = 50000, E_ = 800000;
constexpr int WN = 25;
constexpr int NWIN = 2000;
constexpr int NBK = NWIN / 8;
constexpr int NCH = 256;
constexpr int CH = 3125;
constexpr int INL = 27;
constexpr int CAP = 512;
constexpr int NFRAG = 39;
constexpr int PBIAS = 96;
constexpr float NLOG2E = -1.4426950408889634f;
constexpr float NLN2 = -0.6931471805599453f;

constexpr size_t WS_BLK = 0;
constexpr size_t WS_REC = WS_BLK + (size_t)NBK * NCH * 128;
constexpr size_t WS_POS = WS_REC + (size_t)E_ * 4;
constexpr size_t WS_WPK = WS_POS + (size_t)N_ * 16;
constexpr size_t WS_END = WS_WPK + (size_t)NFRAG * 1024;

constexpr int S_HIST = 0, S_WSUM = 8192, S_BLK = 8192 + 128, S_BYTES = S_BLK + NBK * 128;

__global__ __launch_bounds__(1024) void sort_kernel(const int* __restrict__ src, const int* __restrict__ dst,
                                                    const u16* __restrict__ pos, const u16* __restrict__ we, const u16* __restrict__ wx,
                                                    const u16* __restrict__ wh, char* __restrict__ ws) {
    extern __shared__ __attribute__((aligned(16))) char smem[];
    u32* hist = (u32*)(smem + S_HIST);
    u32* wsum = (u32*)(smem + S_WSUM);
    u32* blk = (u32*)(smem + S_BLK);
    const int tid = threadIdx.x, lane = tid & 63, wv = tid >> 6, c = blockIdx.x;
    const int e0 = c * CH;
    int d[4], s[4];
#pragma unroll
    for (int k = 0; k < 4; ++k) { int i = tid + 1024 * k; i = i < CH ? i : CH - 1; d[k] = dst[e0 + i]; s[k] = src[e0 + i]; }
    hist[tid] = 0; hist[tid + 1024] = 0;

    {
        int n = c * 196 + tid;
        if (tid < 196 && n < N_) {
            f32x4 p = {bf2f(pos[n * 3]), bf2f(pos[n * 3 + 1]), bf2f(pos[n * 3 + 2]), 0.f};
            *(f32x4*)(ws + WS_POS + (size_t)n * 16) = p;
        }
        if (c < NFRAG && wv == 15) {
            const int r = lane & 31, h = lane >> 5;
            const u16* sp; int base, perm = 0, mode = 0;
            if (c < 10) { const int t = c / 5, st = c % 5; sp = we; base = (st < 4 ? 64 + 16 * st : 129) * 64 + 32 * t; mode = 1; }
            else if (c < 18) { sp = we; base = 16 * ((c - 10) & 3) * 64 + 32 * ((c - 10) >> 2); }
            else if (c < 26) { sp = wh; base = 16 * ((c - 18) & 3) * 64 + 32 * ((c - 18) >> 2); }
            else if (c < 34) { sp = wh; base = (64 + 16 * ((c - 26) & 3)) * 64 + 32 * ((c - 26) >> 2); perm = 1; }
            else if (c == 34) { sp = we; base = 128 * 64; mode = 2; }
            else { sp = wx; base = 16 * (c - 35); perm = 1; mode = 3; }
            u16 v[8];
#pragma unroll
            for (int j = 0; j < 8; ++j) {
                const int k = perm ? 8 * (j >> 2) + 4 * h + (j & 3) : 8 * h + j;
                const int idx = mode == 2 ? 32 * ((j >> 1) & 1) + r : (mode == 3 ? k : k * 64 + r);
                v[j] = sp[base + idx];
            }
            const bool on = ((lane & 15) & 7) == 4 * ((lane >> 4) & 1);
#pragma unroll
            for (int j = 0; j < 8; ++j) {
                const float sc = NLOG2E * bf2f(v[j]);
                const u16 hi = f2bf(sc);
                const u16 lo = f2bf(sc - bf2f(hi));
                v[j] = mode == 0 ? v[j] : mode == 1 ? hi : mode == 2 ? ((j & 1) ? lo : hi) : (on ? v[j] : (u16)0);
            }
            u32x4 o = {v[0] | ((u32)v[1] << 16), v[2] | ((u32)v[3] << 16), v[4] | ((u32)v[5] << 16), v[6] | ((u32)v[7] << 16)};
            *(u32x4*)(ws + WS_WPK + (size_t)c * 1024 + lane * 16) = o;
        }
    }
    __syncthreads();
    int wn[4]; u32 rk[4];
#pragma unroll
    for (int k = 0; k < 4; ++k) {
        wn[k] = (u32)d[k] / (u32)WN;
        if (tid + 1024 * k < CH) rk[k] = atomicAdd(&hist[wn[k]], 1u);
    }
    __syncthreads();
    const u32 h0 = hist[2 * tid], h1 = hist[2 * tid + 1];
    const u32 x = wave_scan(h0 + h1);
    if (lane == 63) wsum[wv] = x;
    __syncthreads();
    const u32 wsv = wsum[lane & 15];
    const u32 wsc = wave_scan(lane < 16 ? wsv : 0u) - wsv;
    const u32 pre = __builtin_amdgcn_readlane(wsc, __builtin_amdgcn_readfirstlane(wv));
    const u32 st0 = pre + x - h0 - h1, st1 = st0 + h0;
    const u32 bs = (u32)__shfl((int)st0, lane & ~3);
    *(u32x2*)(hist + 2 * tid) = (u32x2){st0 | ((st0 - bs) << 16), st1 | ((st1 - bs) << 16)};
    if (tid < NWIN / 2) blk[(tid >> 2) * 32 + (tid & 3)] = st0 | (st1 << 16);
    if ((tid & 3) == 0 && tid > 0 && tid <= NWIN / 2) blk[((tid >> 2) - 1) * 32 + 4] = st0;
    __syncthreads();
#pragma unroll
    for (int k = 0; k < 4; ++k) {
        if (tid + 1024 * k < CH) {
            const u32 hv_ = hist[wn[k]];
            const u32 p = (hv_ & 0xFFFFu) + rk[k], j = (hv_ >> 16) + rk[k];
            const u32 r = (u32)s[k] * (u32)(WN * CH) + (u32)(d[k] - wn[k] * WN) * (u32)CH + (u32)(tid + 1024 * k);
            if (j < (u32)INL) blk[(wn[k] >> 3) * 32 + 5 + j] = r; else ((u32*)(ws + WS_REC))[e0 + p] = r;
        }
    }
    __syncthreads();
#pragma unroll
    for (int k = 0; k < 2; ++k) {
        const int i = tid + 1024 * k;
        if (i < NBK * 8) *(u32x4*)(ws + WS_BLK + ((size_t)(i >> 3) * NCH + c) * 128 + (i & 7) * 16) = ((const u32x4*)blk)[i];
    }

}

constexpr int HB = 4160, L_H = 0, L_M = 2 * HB, L_S = L_M + 2048, L_IDX = L_S + 2080, L_SRC = L_IDX + 2048, L_WAVE = L_SRC + 1024 + 224;
constexpr int B_LUT = 8 * L_WAVE, LUTP = 80, B_PRG = B_LUT + 2080, B_STG = B_LUT + 2176, B_BYTES = B_STG + NCH * 128;

static DI void dma16(u32 voff, __amdgpu_buffer_rsrc_t rs, u32 ldsaddr) {
    asm volatile("s_mov_b32 m0, %2\n\ts_nop 0\n\tbuffer_load_dwordx4 %0, %1, 0 offen lds"
                 :: "v"(voff), "s"(rs), "s"(ldsaddr) : "memory", "m0");
}
static DI void dma16p(u32 voff, __amdgpu_buffer_rsrc_t rs, u32 ldsaddr) {
    u32 keep;
    asm volatile("s_nop 4\n\ts_mov_b32 %0, m0\n\ts_mov_b32 m0, %3\n\ts_nop 0\n\tbuffer_load_dwordx4 %1, %2, 0 offen lds\n\ts_mov_b32 m0, %0"
                 : "=&s"(keep) : "v"(voff), "s"(rs), "s"(ldsaddr) : "memory");
}
static DI u32x4 bload16(u32 voff, __amdgpu_buffer_rsrc_t rs) {
    u32x4 r;
    asm volatile("buffer_load_dwordx4 %0, %1, %2, 0 offen" : "=v"(r) : "v"(voff), "s"(rs) : "memory"); return r;
}
typedef u32 u32x3 __attribute__((ext_vector_type(3)));
static DI u32x3 bload12(u32 voff, __amdgpu_buffer_rsrc_t rs) {
    u32x3 r;
    asm volatile("s_mov_b32 exec_lo, 0\n\tbuffer_load_dwordx3 %0, %1, %2, 0 offen\n\ts_mov_b32 exec_lo, -1" : "=v"(r) : "v"(voff), "s"(rs) : "memory"); return r;
}
static DI s16x4 trread(LAS const char* p) {
    typedef short v4i16_t __attribute__((ext_vector_type(4)));
    return __builtin_bit_cast(s16x4, __builtin_amdgcn_ds_read_tr16_b64_v4i16((LAS v4i16_t*)p));
}
#define CBAR() asm volatile("" ::: "memory")
static DI int lane_id() { int l; asm volatile("v_mbcnt_lo_u32_b32 %0, -1, 0\n\tv_mbcnt_hi_u32_b32 %0, -1, %0" : "=v"(l)); return l; }
static DI bf16x8 cat8(s16x4 a, s16x4 b) { return __builtin_shufflevector(a, b, 0, 1, 2, 3, 4, 5, 6, 7); }
static DI float smul(float a, float b) { float r = a * b; asm volatile("" : "+v"(r)); return r; }
static DI bf16x8 asb(u32x4 v) { return __builtin_bit_cast(bf16x8, v); }

__global__ __launch_bounds__(512, 2) void edge_kernel(const u16* __restrict__ hg, const u16* __restrict__ posg, const u16* __restrict__ eag,
                                                      char* __restrict__ ws, u16* __restrict__ out) {
    extern __shared__ __attribute__((aligned(16))) char smem[];
    const int lane = threadIdx.x & 63, e = lane & 31, hh = lane >> 5;
    const int wv = __builtin_amdgcn_readfirstlane(threadIdx.x >> 6);
    const int wl = wv;
    LAS char* B = (LAS char*)smem;
    LAS char* L = B + wv * L_WAVE;
    const u32 lbase = (u32)(uintptr_t)L;
    LAS u32* lidx = (LAS u32*)(L + L_IDX);
    LAS u16* lsrc = (LAS u16*)(L + L_SRC);
    LAS u32* lloc = (LAS u32*)(L + L_M);

    const u32* rec = (const u32*)(ws + WS_REC);
    const u32x4* wpk = (const u32x4*)(ws + WS_WPK);

    {
        const auto rs_b = __builtin_amdgcn_make_buffer_rsrc((void*)(ws + WS_BLK + (size_t)blockIdx.x * NCH * 128), 0, NCH * 128, 0x00020000);
        const u32 vo = (u32)wv * 4096 + (lane >> 3) * 128 + (((lane & 7) ^ ((lane >> 3) & 7)) << 4);
        const u32 sb = (u32)(uintptr_t)(B + B_STG) + wv * 4096;
#pragma unroll
        for (int q = 0; q < 4; ++q) dma16p(vo + q * 1024, rs_b, sb + q * 1024);
    }
    {
        const auto rs_w = __builtin_amdgcn_make_buffer_rsrc((void*)(ws + WS_WPK), 0, NFRAG * 1024, 0x00020000);
#pragma unroll
        for (int q = 0; q < 3; ++q) {
            const int i = wv * 3 + q, f = i < 18 ? i : i + 16;
            dma16p((u32)f * 1024 + lane * 16, rs_w, (u32)(uintptr_t)B + (i >> 2) * L_WAVE + HB + (i & 3) * 1024);
        }
    }
    f32x16 y0 = {0}, y1 = {0}; float yp[3] = {0.f, 0.f, 0.f};
    bf16x8 wf[10], an[4], wxf[4], hw[4];
    {
#pragma unroll
        for (int i = 0; i < 4; ++i) *(LAS u32x4*)(L + L_M + i * 1024 + lane * 16) = (u32x4){0, 0, 0, 0};
        if (lane < 2) *(LAS u32x4*)(L + L_M + 4096 + lane * 16) = (u32x4){0, 0, 0, 0};
        if (threadIdx.x < 26) {
            const int n = threadIdx.x;
#pragma unroll
            for (int i = 0; i < 4; ++i) *(LAS u32x4*)(B + B_LUT + n * LUTP + i * 16) = (u32x4){0, 0, 0, 0};
            CBAR();
            *(LAS u16*)(B + B_LUT + n * LUTP + (2 * (n >> 4) + ((n >> 2) & 1)) * 16 + (((n >> 3) & 1) * 4 + (n & 3)) * 2) = n < WN ? (u16)0x3F80 : (u16)0;
        }
        if (lane == 0) { *(LAS int*)(B + B_PRG + wv * 4) = 0; *(LAS int*)(B + B_PRG + 48 + wv * 4) = 0; }
        if (threadIdx.x < 2) *(LAS int*)(B + B_PRG + 32 + threadIdx.x * 4) = 0;
        asm volatile("s_waitcnt vmcnt(0) lgkmcnt(0)\n\ts_barrier" ::: "memory");
    }
    const int nbase = (blockIdx.x * 8 + wl) * WN;
    u32x4 hwr[4], pwr;
    {
        const int nn = nbase + (e < WN ? e : WN - 1);
        const auto rs_h0 = __builtin_amdgcn_make_buffer_rsrc((void*)hg, 0, N_ * 128, 0x00020000);
        const auto rs_p0 = __builtin_amdgcn_make_buffer_rsrc((void*)(ws + WS_POS), 0, N_ * 16, 0x00020000);
#pragma unroll
        for (int s = 0; s < 4; ++s) hwr[s] = bload16((u32)nn * 128 + 32 * s + 16 * hh, rs_h0);
        pwr = bload16((u32)nn * 16, rs_p0);
    }
    const u32 fsw = (e >> 1) & 7;
    LAS const char* hrd[2];
#pragma unroll
    for (int s = 0; s < 2; ++s) hrd[s] = L + L_H + (e >> 3) * 1040 + (e & 7) * 128 + ((s ^ ((e >> 1) & 1)) << 6) + hh * 16;
    const u32 swz0 = ((lane & 7) << 4) ^ (((lane >> 4) & 1) << 6);
    LAS char* mwr[4];
#pragma unroll
    for (int g = 0; g < 4; ++g) mwr[g] = L + L_M + e * 64 + (((2 * g + hh) ^ fsw) << 3);
    const int i16 = lane & 15, tq = i16 >> 2, tp = i16 & 3, tb = (lane >> 4) & 1;
    LAS const char* mrd[4];
#pragma unroll
    for (int i = 0; i < 4; ++i) {
        const int row = 16 * (i >> 1) + 8 * hh + 4 * (i & 1) + tq;
        mrd[i] = L + L_M + row * 64 + (((4 * tb + tp) ^ ((row >> 1) & 7)) << 3);
    }
#pragma unroll
    for (int i = 0; i < 4; ++i) asm volatile("" : "+v"(mrd[i]), "+v"(mwr[i]));
    LAS const char* srd = L + L_S + e * 80 + hh * 16;
    const u32 lutd = (u32)(B_LUT - L_S - wv * L_WAVE) + hh * 16;

    int T = 0; bool whl = false; u32x2 hv[8];
    for (int b0 = 0; b0 == 0 || b0 < T; b0 += CAP) {
        int nb, nt;
        {
            const int ln = lane_id();
            LAS const char* sg = B + B_STG + ln * 128;
            const u32 sx = (ln & 7) << 4;
            u32 lo_[4], cn_[4], s0_[4];
#pragma unroll
            for (int q = 0; q < 4; ++q) {
                const u32 a = *(LAS const u16*)(sg + q * 8192 + ((2 * wl) ^ sx)), b = *(LAS const u16*)(sg + q * 8192 + ((2 * wl + 2) ^ sx));
                s0_[q] = *(LAS const u16*)(sg + q * 8192 + sx);
                lo_[q] = a; cn_[q] = b - a;
            }
            const u32 ltot = cn_[0] + cn_[1] + cn_[2] + cn_[3];
            const u32 inc = wave_scan(ltot);
            T = __builtin_amdgcn_readlane((int)inc, 63);
            nb = (T - b0) < CAP ? (T - b0) : CAP;
            nt = (nb + 31) >> 5;
            u32 pp[4];
            pp[0] = inc - ltot - (u32)b0; pp[1] = pp[0] + cn_[0]; pp[2] = pp[1] + cn_[1]; pp[3] = pp[2] + cn_[2];
            if (b0 > 0) {
#pragma unroll
                for (int i = 0; i < 2; ++i) *(LAS u32x4*)(L + L_M + i * 1024 + ln * 16) = (u32x4){0, 0, 0, 0};
                CBAR();
            }
#pragma unroll
            for (int q = 0; q < 4; ++q) {
                const int st = (int)pp[q], en = st + (int)cn_[q], s1 = st > 0 ? st : 0;
                if (en > s1 && s1 < CAP) lloc[s1] = ((u32)(s1 + 1) << 20) | (((u32)(ln + 64 * q) << 12) + lo_[q] - s0_[q] + (u32)(s1 - st));
            }
            CBAR();
#define DECODE(K0, K1) { \
                const int ln = lane_id(); \
                u32 lo2[8], r[8]; bool ovf = false; \
                _Pragma("unroll") for (int k = K0; k < K1; ++k) lo2[k] = lloc[64 * k + ln]; \
                _Pragma("unroll") for (int k = K0; k < K1; ++k) { \
                    u32 v = max(wave_maxscan(lo2[k]), carry); \
                    carry = __builtin_amdgcn_readlane(v, 63); \
                    v = (v & 0xFFFFFu) + (u32)(64 * k + 1 + ln) - (v >> 20); \
                    lo2[k] = 64 * k + ln < nb ? v : 0u; } \
                _Pragma("unroll") for (int k = K0; k < K1; ++k) { \
                    const u32 cc = lo2[k] >> 12, jj = lo2[k] & 4095; \
                    r[k] = *(LAS const u32*)(B + B_STG + cc * 128 + ((20 + 4 * (jj < (u32)INL ? jj : 0u)) ^ ((cc & 7) << 4))); \
                    ovf |= jj >= (u32)INL; \
                } \
                if (__builtin_amdgcn_ballot_w64(ovf)) { \
                    _Pragma("unroll") for (int k = K0; k < K1; ++k) { \
                        const u32 cc = lo2[k] >> 12, jj = lo2[k] & 4095; \
                        if (jj >= (u32)INL) r[k] = rec[cc * CH + *(LAS const u16*)(B + B_STG + cc * 128 + ((cc & 7) << 4)) + jj]; \
                    } \
                    _Pragma("unroll") for (int k = K0; k < K1; ++k) asm volatile("" : "+v"(r[k])); \
                } \
                _Pragma("unroll") for (int k = K0; k < K1; ++k) { \
                    const int i = 64 * k + ln; \
                    const u32 sv = r[k] / (u32)(WN * CH), rm = r[k] - sv * (u32)(WN * CH), dv = rm / (u32)CH; \
                    const bool ok = i < nb, hi_ = k > 0 && ok && sv >= (u32)(N_ / 2); \
                    const unsigned long long mh_ = __builtin_amdgcn_ballot_w64(hi_); \
                    const int bh_ = __builtin_amdgcn_mbcnt_hi((u32)(mh_ >> 32), __builtin_amdgcn_mbcnt_lo((u32)mh_, 0u)) + nhi; \
                    const int p_ = hi_ ? nb - 1 - bh_ : (ok ? i - bh_ : i); \
                    nhi += __builtin_popcountll(mh_); \
                    lidx[p_] = ok ? (((lo2[k] >> 12) * CH + rm - dv * (u32)CH) << 5) | (dv << 27) : ((u32)WN << 27); \
                    lsrc[p_] = ok ? (u16)sv : (u16)0; \
                } \
                CBAR(); }
        }
        int nhi = 0; u32 carry = 0;
        DECODE(0, 1)
        CBAR();

        u32x4 eaA, eaB; u32x3 psA, psB;
        const int ln2 = lane_id();
        LAS const u16* lsr8 = lsrc + (ln2 >> 3);
        LAS const u16* lsre = lsrc + (ln2 & 31);
        LAS const u32* lide = lidx + (ln2 & 31);
#define READ_D(TT) { const int tb_ = (TT) * 32; s0_ = lsr8[tb_]; s1_ = lsr8[tb_ + 8]; s2_ = lsr8[tb_ + 16]; s3_ = lsr8[tb_ + 24]; }
#define READ_E(TT) { li_ = lide[(TT) * 32]; so_ = lsre[(TT) * 32]; }
#define ISSUE_D(TT, BUF) do { \
            const auto rs_h = __builtin_amdgcn_make_buffer_rsrc((void*)hg, 0, ((TT) < nt) ? N_ * 128 : 0, 0x00020000); \
            dma16(s0_ * 128 + swz0, rs_h, lbase + L_H + (BUF) * HB); \
            dma16(s1_ * 128 + swz0, rs_h, lbase + L_H + (BUF) * HB + 1040); \
            dma16(s2_ * 128 + swz0, rs_h, lbase + L_H + (BUF) * HB + 2080); \
            dma16(s3_ * 128 + swz0, rs_h, lbase + L_H + (BUF) * HB + 3120); \
        } while (0)
#define ISSUE_E(TT, EA, PS) do { \
            const auto rs_e = __builtin_amdgcn_make_buffer_rsrc((void*)eag, 0, ((TT) < nt) ? E_ * 32 : 0, 0x00020000); \
            const auto rs_p = __builtin_amdgcn_make_buffer_rsrc((void*)(ws + WS_POS), 0, ((TT) < nt) ? N_ * 16 : 0, 0x00020000); \
            EA = bload16((li_ & 0x1FFFFFFu) | (hh * 16), rs_e); \
            PS = bload12(so_ * 16, rs_p); \
        } while (0)
#define MFMA16(a, b, c) __builtin_amdgcn_mfma_f32_16x16x32_bf16((a), (b), (c), 0, 0, 0)
#define SGROUP(C, PK, G, SA, SB, SC) do { \
            float e0_, e1_, e2_, e3_; \
            asm volatile("v_exp_f32 %0, %4\n\tv_exp_f32 %1, %5\n\tv_exp_f32 %2, %6\n\tv_exp_f32 %3, %7" \
                         : "=&v"(e0_), "=&v"(e1_), "=&v"(e2_), "=&v"(e3_) : "v"(C[4 * (G)]), "v"(C[4 * (G) + 1]), "v"(C[4 * (G) + 2]), "v"(C[4 * (G) + 3])); \
            SA \
            asm volatile("v_add_f32 %0, 1.0, %0\n\tv_add_f32 %1, 1.0, %1\n\tv_add_f32 %2, 1.0, %2\n\tv_rcp_f32 %0, %0\n\tv_add_f32 %3, 1.0, %3\n\tv_rcp_f32 %1, %1" \
                         : "+v"(e0_), "+v"(e1_), "+v"(e2_), "+v"(e3_)); \
            SB \
            asm volatile("v_rcp_f32 %2, %2\n\tv_rcp_f32 %3, %3\n\tv_mul_f32 %0, %0, %4\n\tv_mul_f32 %1, %1, %5\n\tv_mul_f32 %2, %2, %6\n\tv_mul_f32 %3, %3, %7\n\t" \
                         "v_cvt_pk_bf16_f32 %0, %0, %1\n\tv_cvt_pk_bf16_f32 %2, %2, %3" \
                         : "+v"(e0_), "+v"(e1_), "+v"(e2_), "+v"(e3_) : "v"(C[4 * (G)]), "v"(C[4 * (G) + 1]), "v"(C[4 * (G) + 2]), "v"(C[4 * (G) + 3])); \
            PK[2 * (G)] = __float_as_uint(e0_); PK[2 * (G) + 1] = __float_as_uint(e2_); \
            SC \
        } while (0)
#define M32(D, A_, B_) { asm volatile("" : "+v"(D)); D = MFMA(A_, B_, D); asm volatile("" : "+v"(D)); }
#define M16(D, A_, B_) { asm volatile("" : "+v"(D)); D = MFMA16(A_, B_, D); asm volatile("" : "+v"(D)); }
#define M32Z(D, A_, B_, P_) { asm volatile("" : "+v"(P_)); D = MFMA(A_, B_, zero16); asm volatile("" : "+v"(D)); }
#define M16Z(D, A_, B_, P_) { asm volatile("" : "+v"(P_)); D = MFMA16(A_, B_, zero4); asm volatile("" : "+v"(D)); }
#define FETCH(TT, BUF, EA, PS, GN) \
            asm volatile("s_waitcnt vmcnt(6)" : "+v"(EA), "+v"(PS) :: "memory"); \
            dl = dln < WN ? dln : WN; \
            _Pragma("unroll") for (int s = 0; s < 4; ++s) xb[s] = *(LAS const bf16x8*)(hrd[s >> 1] + (BUF) * HB + (s & 1) * 32); \
            srow = L + L_S + dl * 80; \
            { LAS const char* lp_ = srow + lutd; \
              oh0 = *(LAS const u32x4*)lp_; oh1 = *(LAS const u32x4*)(lp_ + 32); \
              const f32x4 pd_ = *(LAS const f32x4*)(srow + 64); GN[0] = pd_[0]; GN[1] = pd_[1]; GN[2] = pd_[2]; } \
            dln = lide[((TT) + 1) * 32] >> 27;
#define GEOM(PS, GN) \
            GN[0] -= __uint_as_float(PS[0]); asm volatile("" : "+v"(GN[0])); GN[1] -= __uint_as_float(PS[1]); asm volatile("" : "+v"(GN[1])); GN[2] -= __uint_as_float(PS[2]); \
            { const float d2 = __builtin_fmaf(GN[0], GN[0], __builtin_fmaf(GN[1], GN[1], GN[2] * GN[2])); const u32 dp_ = cvtpk(d2, d2); oh1[3] = hh ? dp_ : oh1[3]; }

        const f32x16 zero16 = {0};
        f32x16 c0, c1; bf16x8 xb[4]; u32x4 oh0, oh1;
        bf16x8 sf0 = {0}, sf1 = {0}; s16x4 u0 = {0}, u1 = {0}, u2 = {0}, u3 = {0};
        const f32x4 zero4 = {0.f, 0.f, 0.f, 0.f}; f32x4 cz = zero4; u32 pk1[8] = {0u, 0u, 0u, 0u, 0u, 0u, 0u, 0u};
        float gA[3], gB[3] = {0.f, 0.f, 0.f}; u32 dl, dln; LAS char* srow;

        u32 s0_, s1_, s2_, s3_, li_, so_;
        READ_D(0) READ_E(0) ISSUE_D(0, 0); ISSUE_E(0, eaA, psA); READ_E(1) ISSUE_E(1, eaB, psB);
        DECODE(1, 7)
        if (nb > 448) DECODE(7, 8)
        if (b0 == 0) {
        asm volatile("s_waitcnt vmcnt(8)" : "+v"(hwr[0]), "+v"(hwr[1]), "+v"(hwr[2]), "+v"(hwr[3]), "+v"(pwr) :: "memory");
#pragma unroll
        for (int s = 0; s < 4; ++s) hw[s] = asb(hwr[s]);
        if (lane < WN) *(LAS u32x4*)(L + L_S + lane * 80 + 64) = pwr;
#define WFR(i) (*(LAS const u32x4*)(B + ((i) >> 2) * L_WAVE + HB + ((i) & 3) * 1024 + lane * 16))
        f32x16 a0 = {0}, a1 = {0};
#pragma unroll
        for (int s = 0; s < 4; ++s) {
            a0 = MFMA(hw[s], asb(WFR(10 + s)), a0);
            a1 = MFMA(hw[s], asb(WFR(14 + s)), a1);
        }
        const u32x4 wd = WFR(18);
#pragma unroll
        for (int i = 0; i < 10; ++i) wf[i] = asb(WFR(i));
#pragma unroll
        for (int i = 0; i < 4; ++i) wxf[i] = asb(WFR(19 + i));
        u32 p0[8], p1[8];
#pragma unroll
        for (int i = 0; i < 8; ++i) { p0[i] = cvtpk(a0[2 * i] * NLOG2E, a0[2 * i + 1] * NLOG2E); p1[i] = cvtpk(a1[2 * i] * NLOG2E, a1[2 * i + 1] * NLOG2E); }
        if (hh) { p0[7] = wd[0]; p1[7] = wd[1]; }
        an[0] = asb((u32x4){p0[0], p0[1], p0[2], p0[3]}); an[1] = asb((u32x4){p0[4], p0[5], p0[6], p0[7]});
        an[2] = asb((u32x4){p1[0], p1[1], p1[2], p1[3]}); an[3] = asb((u32x4){p1[4], p1[5], p1[6], p1[7]});
#pragma unroll
        for (int i = 0; i < 10; ++i) asm volatile("" : "+v"(wf[i]));
#pragma unroll
        for (int i = 0; i < 4; ++i) asm volatile("" : "+v"(wxf[i]), "+v"(an[i]));
        if (T > CAP) *(LAS volatile int*)(B + B_PRG + 32) = 1;
        asm volatile("s_waitcnt lgkmcnt(0)\n\ts_barrier" ::: "memory");
        whl = __builtin_amdgcn_readfirstlane(*(LAS volatile int*)(B + B_PRG + 32)) == 0;
        {
            const auto rs_w = __builtin_amdgcn_make_buffer_rsrc((void*)(ws + WS_WPK), 0, whl ? NFRAG * 1024 : 0, 0x00020000);
            const u32 dsta = whl ? (u32)(uintptr_t)(B + B_STG) + wv * 2048 : lbase + L_H + HB;
            dma16p((u32)(18 + 2 * wv) * 1024 + lane * 16, rs_w, dsta);
            dma16p((u32)(19 + 2 * wv) * 1024 + lane * 16, rs_w, dsta + 1024);
        }
        }
        READ_D(1) ISSUE_D(1, 1);
        dln = lide[0] >> 27;
        READ_D(2) READ_E(2)
        FETCH(0, 0, eaA, psA, gA)
        GEOM(psA, gA)
        asm volatile("" : "+v"(xb[0]), "+v"(xb[1]), "+v"(xb[2]), "+v"(xb[3]) :: "memory");
        ISSUE_D(2, 0);
        c0 = MFMA(wf[4], asb(eaA), zero16);
#pragma unroll
        for (int s = 0; s < 4; ++s) c0 = MFMA(wf[s], xb[s], c0);
        c0 = MFMA(an[0], asb(oh0), c0);
        c0 = MFMA(an[1], asb(oh1), c0);
        asm volatile("s_nop 7\n\ts_nop 7\n\ts_nop 3" : "+v"(c0));

#define PKV(P, O) asb((u32x4){P[O], P[(O) + 1], P[(O) + 2], P[(O) + 3]})
#define POSV(GP) { asm volatile("" : "+v"(cz)); const float csv = cz[0] * NLN2; \
              *(LAS u32x2*)mwr[0] = (u32x2){cvtpk(smul(csv, GP[0]), smul(csv, GP[1])), cvtpk(smul(csv, GP[2]), 0.f)}; \
              CBAR(); \
              q0 = trread(mrd[0]); q1 = trread(mrd[1]); q2 = trread(mrd[2]); q3 = trread(mrd[3]); \
              CBAR(); }
#define BODY(TT, BUF, EA, PS, EAN, PSN, GN) do { \
            u32 pk0[8]; s16x4 q0, q1, q2, q3; f32x16 yt; \
            LAS u16* sp_ = (LAS u16*)(srow + e * 2); \
            *sp_ = (u16)0x3F80; \
            CBAR(); \
              \
            SGROUP(c0, pk0, 0, M16(cz, wxf[3], PKV(pk1, 4)), M32Z(c1, wf[9], asb(EA), EA), M32(c1, wf[5], xb[0])); \
            ISSUE_E((TT) + 2, EA, PS); \
            POSV(GN); \
            SGROUP(c0, pk0, 1, M32(c1, wf[6], xb[1]), M32(y1, cat8(u0, u1), sf0), M32(c1, wf[7], xb[2])); \
            SGROUP(c0, pk0, 2, M32(y1, cat8(u2, u3), sf1), M32(c1, wf[8], xb[3]), M32(c1, an[2], asb(oh0))); \
            SGROUP(c0, pk0, 3, M32(c1, an[3], asb(oh1)), M32Z(yt, cat8(q0, q1), sf0, sf0), M32(yt, cat8(q2, q3), sf1)); \
            CBAR(); \
            sf0 = *(LAS const bf16x8*)srd; sf1 = *(LAS const bf16x8*)(srd + 32); \
            CBAR(); \
            *sp_ = (u16)0; \
            _Pragma("unroll") for (int g = 0; g < 4; ++g) *(LAS u32x2*)mwr[g] = (u32x2){pk0[2 * g], pk0[2 * g + 1]}; \
            CBAR(); \
            const s16x4 t0 = trread(mrd[0]), t1 = trread(mrd[1]), t2 = trread(mrd[2]), t3 = trread(mrd[3]); \
            CBAR(); \
              \
            SGROUP(c1, pk1, 0, M16Z(cz, wxf[0], PKV(pk0, 0), pk0[0]), M16(cz, wxf[1], PKV(pk0, 4)), \
                   { asm volatile("" : "+v"(yt)); yp[0] += yt[0]; yp[1] += yt[1]; yp[2] += yt[2]; asm volatile("" : "+v"(yp[0]), "+v"(yp[1]), "+v"(yp[2])); } ); \
            FETCH((TT) + 1, (BUF) ^ 1, EAN, PSN, GN) \
            READ_D((TT) + 3) \
            M32Z(c0, wf[4], asb(EAN), EAN) \
            SGROUP(c1, pk1, 1, M32(y0, cat8(t0, t1), sf0), M32(y0, cat8(t2, t3), sf1), M32(c0, wf[0], xb[0])); \
            GEOM(PSN, GN) \
            asm volatile("" : "+v"(xb[0]), "+v"(xb[1]), "+v"(xb[2]), "+v"(xb[3]) :: "memory"); \
            ISSUE_D((TT) + 3, (BUF) ^ 1); \
            SGROUP(c1, pk1, 2, M32(c0, wf[1], xb[1]), M32(c0, wf[2], xb[2]), M32(c0, wf[3], xb[3])); \
            SGROUP(c1, pk1, 3, M32(c0, an[0], asb(oh0)), M32(c0, an[1], asb(oh1)), M16(cz, wxf[2], PKV(pk1, 0))); \
            CBAR(); \
            _Pragma("unroll") for (int g = 0; g < 4; ++g) *(LAS u32x2*)mwr[g] = (u32x2){pk1[2 * g], pk1[2 * g + 1]}; \
            CBAR(); \
            READ_E((TT) + 3) \
            u0 = trread(mrd[0]); u1 = trread(mrd[1]); u2 = trread(mrd[2]); u3 = trread(mrd[3]); \
            CBAR(); \
        } while (0)

        LAS int* prg = (LAS int*)(B + B_PRG);
        int other = 0;
#pragma clang loop unroll(disable)
        for (int t = 0; t < nt; t += 2) {
            if (T - b0 - 32 * t + PBIAS > __builtin_amdgcn_readfirstlane(other)) __builtin_amdgcn_s_setprio(1); else __builtin_amdgcn_s_setprio(0);
            prg[wv] = T - b0 - 32 * t;
            other = prg[wv ^ 4];
            BODY(t, 0, eaA, psA, eaB, psB, gB);
            prg[12 + wv] = 1;
            if (t + 1 < nt) BODY(t + 1, 1, eaB, psB, eaA, psA, gA);
        }
        prg[wv] = T - b0 - 32 * nt; __builtin_amdgcn_s_setprio(0);
        {
            CBAR();
            const int l3 = lane_id(), e3 = l3 & 31, h3 = l3 >> 5;
            const int nn = nbase + (e3 < WN ? e3 : WN - 1);
#pragma unroll
            for (int s = 0; s < 4; ++s) hw[s] = *(const bf16x8*)(hg + (size_t)nn * 64 + 16 * s + 8 * h3);
#pragma unroll
            for (int g = 0; g < 8; ++g) hv[g] = *(const u32x2*)(hg + (size_t)nn * 64 + 32 * (g >> 2) + 8 * (g & 3) + 4 * h3);
            CBAR();
        }
        {
            s16x4 q0, q1, q2, q3;
            cz = MFMA16(wxf[3], PKV(pk1, 4), cz);
            y1 = MFMA(cat8(u0, u1), sf0, y1);
            y1 = MFMA(cat8(u2, u3), sf1, y1);
            CBAR();
            float gl[3];
#pragma unroll
            for (int i = 0; i < 3; ++i) gl[i] = (nt & 1) ? gA[i] : gB[i];
            POSV(gl);
            f32x16 yt = MFMA(cat8(q0, q1), sf0, zero16);
            yt = MFMA(cat8(q2, q3), sf1, yt);
            yp[0] += yt[0]; yp[1] += yt[1]; yp[2] += yt[2];
        }
        asm volatile("s_waitcnt vmcnt(0)" : "+v"(eaA), "+v"(psA), "+v"(eaB), "+v"(psB) :: "memory");
        asm volatile("" : "+v"(hw[0]), "+v"(hw[1]), "+v"(hw[2]), "+v"(hw[3]));
        asm volatile("" : "=v"(hwr[0]), "=v"(hwr[1]), "=v"(hwr[2]), "=v"(hwr[3]), "=v"(pwr));
        asm volatile("" : "+v"(hv[0]), "+v"(hv[1]), "+v"(hv[2]), "+v"(hv[3]), "+v"(hv[4]), "+v"(hv[5]), "+v"(hv[6]), "+v"(hv[7]));
        prg[12 + wv] = 1;
    }

    {
        CBAR();
        const int lane = lane_id();
        const int e = lane & 31, hh = lane >> 5;
        const int nn = nbase + (e < WN ? e : WN - 1);
        u32 p0[8], p1[8];
#pragma unroll
        for (int i = 0; i < 8; ++i) { p0[i] = cvtpk(y0[2 * i] * NLN2, y0[2 * i + 1] * NLN2); p1[i] = cvtpk(y1[2 * i] * NLN2, y1[2 * i + 1] * NLN2); }
        bf16x8 mb[4];
        mb[0] = asb((u32x4){p0[0], p0[1], p0[2], p0[3]});
        mb[1] = asb((u32x4){p0[4], p0[5], p0[6], p0[7]});
        mb[2] = asb((u32x4){p1[0], p1[1], p1[2], p1[3]});
        mb[3] = asb((u32x4){p1[4], p1[5], p1[6], p1[7]});
        f32x16 o0 = {0}, o1 = {0};
        if (__builtin_amdgcn_ballot_w64(*(LAS volatile int*)(B + B_PRG + 48 + (lane & 7) * 4) == 0)) whl = false;
        const u32x4* whp = whl ? (const u32x4*)(char*)(B + B_STG) + lane : wpk + 18 * 64 + lane;
#pragma unroll
        for (int s = 0; s < 4; ++s) {
            o0 = MFMA(asb(whp[(8 + s) * 64]), mb[s], o0);
            o1 = MFMA(asb(whp[(12 + s) * 64]), mb[s], o1);
        }
#pragma unroll
        for (int s = 0; s < 4; ++s) {
            o0 = MFMA(asb(whp[s * 64]), hw[s], o0);
            o1 = MFMA(asb(whp[(4 + s) * 64]), hw[s], o1);
        }
        if (e < WN) {
#pragma unroll
            for (int g = 0; g < 8; ++g) {
                const int f = 32 * (g >> 2) + 8 * (g & 3) + 4 * hh;
                float r[4];
#pragma unroll
                for (int i = 0; i < 4; ++i) {
                    const float a = g < 4 ? o0[4 * (g & 3) + i] : o1[4 * (g & 3) + i];
                    const float sv = a * __builtin_amdgcn_rcpf(1.f + __builtin_amdgcn_exp2f(a * NLOG2E));
                    const u32 hw_ = hv[g][i >> 1];
                    const float hf = __uint_as_float((i & 1) ? (hw_ & 0xFFFF0000u) : (hw_ << 16));
                    r[i] = hf + sv;
                }
                *(u32x2*)(out + (size_t)nn * 64 + f) = (u32x2){cvtpk(r[0], r[1]), cvtpk(r[2], r[3])};
            }
        }
        {
            const float ox = yp[0], oy = yp[1], oz = yp[2];
            if (e < WN && hh == 1) {
                const size_t o = (size_t)nn * 3;
                u16* po = out + (size_t)N_ * 64 + o;
                const f32x4 pw = *(LAS const f32x4*)(L + L_S + e * 80 + 64);
                po[0] = f2bf(pw[0] + ox); po[1] = f2bf(pw[1] + oy); po[2] = f2bf(pw[2] + oz);
            }
        }
    }
}

__global__ __launch_bounds__(256) void g_edge_kernel(const u16* h, const u16* pos, const u16* ea, const int* src, const int* dst,
                                                     const u16* we, const u16* wx, float* macc, float* pacc, int E) {
    __shared__ float sw[145 * 64];
    for (int i = threadIdx.x; i < 145 * 64; i += blockDim.x) sw[i] = bf2f(we[i]);
    __syncthreads();
    int lane = threadIdx.x & 63;
    int wid = (blockIdx.x * blockDim.x + threadIdx.x) >> 6;
    int nw = (gridDim.x * blockDim.x) >> 6;
    float wxl = bf2f(wx[lane]);
    for (int e = wid; e < E; e += nw) {
        int s = src[e], d = dst[e];
        float dx = bf2f(pos[(size_t)d * 3 + 0]) - bf2f(pos[(size_t)s * 3 + 0]);
        float dy = bf2f(pos[(size_t)d * 3 + 1]) - bf2f(pos[(size_t)s * 3 + 1]);
        float dz = bf2f(pos[(size_t)d * 3 + 2]) - bf2f(pos[(size_t)s * 3 + 2]);
        float d2 = dx * dx + dy * dy + dz * dz;
        float hd = bf2f(h[(size_t)d * 64 + lane]), hs = bf2f(h[(size_t)s * 64 + lane]);
        float eav = lane < 16 ? bf2f(ea[(size_t)e * 16 + lane]) : 0.f;
        float acc = d2 * sw[128 * 64 + lane];
#pragma unroll 8
        for (int k = 0; k < 64; ++k) acc += __shfl(hd, k) * sw[k * 64 + lane];
#pragma unroll 8
        for (int k = 0; k < 64; ++k) acc += __shfl(hs, k) * sw[(64 + k) * 64 + lane];
#pragma unroll 8
        for (int k = 0; k < 16; ++k) acc += __shfl(eav, k) * sw[(129 + k) * 64 + lane];
        float m = acc / (1.f + __expf(-acc));
        float cs = m * wxl;
        for (int o = 32; o > 0; o >>= 1) cs += __shfl_xor(cs, o);
        atomicAdd(&macc[(size_t)d * 64 + lane], m);
        if (lane < 3) atomicAdd(&pacc[(size_t)d * 3 + lane], (lane == 0 ? dx : lane == 1 ? dy : dz) * cs);
    }
}
__global__ __launch_bounds__(256) void g_node_kernel(const u16* h, const u16* pos, const u16* wh, const float* macc, const float* pacc,
                                                     u16* out, int N) {
    __shared__ float sw[128 * 64];
    for (int i = threadIdx.x; i < 128 * 64; i += blockDim.x) sw[i] = bf2f(wh[i]);
    __syncthreads();
    int lane = threadIdx.x & 63;
    int wid = (blockIdx.x * blockDim.x + threadIdx.x) >> 6;
    int nw = (gridDim.x * blockDim.x) >> 6;
    for (int n = wid; n < N; n += nw) {
        float hv = bf2f(h[(size_t)n * 64 + lane]);
        float mv = macc[(size_t)n * 64 + lane];
        float acc = 0.f;
#pragma unroll 8
        for (int k = 0; k < 64; ++k) acc += __shfl(hv, k) * sw[k * 64 + lane];
#pragma unroll 8
        for (int k = 0; k < 64; ++k) acc += __shfl(mv, k) * sw[(64 + k) * 64 + lane];
        float s = acc / (1.f + __expf(-acc));
        out[(size_t)n * 64 + lane] = f2bf(hv + s);
        if (lane < 3) out[(size_t)N * 64 + (size_t)n * 3 + lane] = f2bf(bf2f(pos[(size_t)n * 3 + lane]) + pacc[(size_t)n * 3 + lane]);
    }
}

extern "C" void kernel_launch(void* const* d_in, const int* in_sizes, int n_in,
                              void* d_out, int out_size, void* d_ws, size_t ws_size,
                              hipStream_t stream) {
    const u16* h = (const u16*)d_in[0];
    const u16* pos = (const u16*)d_in[1];
    const u16* ea = (const u16*)d_in[2];
    const int* src = (const int*)d_in[3];
    const int* dst = (const int*)d_in[4];
    const u16* we = (const u16*)d_in[5];
    const u16* wx = (const u16*)d_in[6];
    const u16* wh = (const u16*)d_in[7];
    const int N = in_sizes[0] / 64, E = in_sizes[3];
    if (N == N_ && E == E_ && ws_size >= WS_END) {
        (void)hipFuncSetAttribute((const void*)sort_kernel, hipFuncAttributeMaxDynamicSharedMemorySize, S_BYTES);
        (void)hipFuncSetAttribute((const void*)edge_kernel, hipFuncAttributeMaxDynamicSharedMemorySize, B_BYTES);
        sort_kernel<<<NCH, 1024, S_BYTES, stream>>>(src, dst, pos, we, wx, wh, (char*)d_ws);
        edge_kernel<<<NBK, 512, B_BYTES, stream>>>(h, pos, ea, (char*)d_ws, (u16*)d_out);
    } else {
        float* macc = (float*)d_ws;
        float* pacc = macc + (size_t)N * 64;
        (void)hipMemsetAsync(d_ws, 0, (size_t)N * 67 * 4, stream);
        g_edge_kernel<<<2048, 256, 0, stream>>>(h, pos, ea, src, dst, we, wx, macc, pacc, E);
        g_node_kernel<<<1024, 256, 0, stream>>>(h, pos, wh, macc, pacc, (u16*)d_out, N);
    }
}
```
